```python
import torch

def _softmax(x, axis=-1):
    return torch.softmax(x, dim=axis)
def _gelu(x):
    return torch.nn.functional.gelu(x, approximate="tanh")
def _silu(x):
    return torch.nn.functional.silu(x)
def _sigmoid(x):
    return torch.sigmoid(x)
def _layernorm(x, axis=-1, eps=1e-5):
    m = x.mean(dim=axis, keepdim=True)
    v = x.var(dim=axis, keepdim=True, unbiased=False)
    return (x - m) / torch.sqrt(v + eps)
def _scatter_add(tgt, idx, src, axis=0):
    return tgt.clone().index_add_(axis, idx, src)
def _segment_sum(src, seg, n):
    out = torch.zeros((n,) + src.shape[1:], dtype=src.dtype)
    return out.index_add_(0, seg, src)
def _pad(x, pw):
    flat = tuple(v for pair in reversed(pw) for v in pair)
    return torch.nn.functional.pad(x, flat)

kernel_name = 'sequential_preLN__sliding_block_cached__glu_gated_glu__decode'

def setup_inputs():

    _w = (lambda a: (lambda t: t.to(torch.bfloat16) if t.is_floating_point() else t)(torch.as_tensor(a)))
    def _A_setup():
        B, Sq, D = 2, 1, 512
        Hq, Hkv, Sk, Dh = 32, 8, 32768, 128
        assert Hq % Hkv == 0, (Hq, Hkv)
        return {
            "x": _w((lambda *s: torch.randn(*s, dtype=torch.bfloat16) * 0.05)(B, Sq, D)),
            "k_cache": _w((lambda *s: torch.randn(*s, dtype=torch.bfloat16) * 3.2)(B, Hkv, Sk, Dh)),
            "v_cache": _w((lambda *s: torch.randn(*s, dtype=torch.bfloat16) * 0.4)(B, Hkv, Sk, Dh)),
            "wq": _w((lambda *s: torch.randn(*s, dtype=torch.bfloat16) * 3.2)(D, Hq * Dh) * (D ** -0.5)),
            "wo": _w((lambda *s: torch.randn(*s, dtype=torch.bfloat16) * 0.4)(Hq * Dh, D) * ((Hq * Dh) ** -0.5)),
        }
    _A_in = _A_setup()

    def _B_setup():
        B, S, D, F = 2, 1, 512, 1792
        return {
            "x": _w((lambda *s: torch.randn(*s, dtype=torch.bfloat16) * 0.05)(B, S, D)),
            "w_gate": _w((lambda *s: torch.randn(*s, dtype=torch.bfloat16) * 0.1)(D, F)),
            "w_up": _w((lambda *s: torch.randn(*s, dtype=torch.bfloat16) * 0.05)(D, F)),
            "w_down": _w((lambda *s: torch.randn(*s, dtype=torch.bfloat16) * 0.05)(F, D)),
        }
    _B_in = _B_setup()

    return {
        "A_x": _A_in["x"],
        "A_k_cache": _A_in["k_cache"],
        "A_v_cache": _A_in["v_cache"],
        "A_wq": _A_in["wq"],
        "A_wo": _A_in["wo"],
        "B_x": _B_in["x"],
        "B_w_gate": _B_in["w_gate"],
        "B_w_up": _B_in["w_up"],
        "B_w_down": _B_in["w_down"],
    }

def reference(A_x, A_k_cache, A_v_cache, A_wq, A_wo, B_x, B_w_gate, B_w_up, B_w_down):
    def _A_ref(x, k_cache, v_cache, wq, wo):
        B, Sq, D = x.shape
        Hq, Dh = 32, 128
        Hkv, Sk = k_cache.shape[1], k_cache.shape[2]
        W = min(4096, max(256, Sk // 4))
        rep = Hq // Hkv
        q = (lambda x, *d: x.permute(*d))((x @ wq).reshape(B, Sq, Hq, Dh), 0, 2, 1, 3)
        k = (lambda x, n, axis: torch.repeat_interleave(x, n, dim=axis))(k_cache[:, :, Sk - W:, :], rep, axis=1)
        v = (lambda x, n, axis: torch.repeat_interleave(x, n, dim=axis))(v_cache[:, :, Sk - W:, :], rep, axis=1)
        scores = torch.einsum("bhqd,bhkd->bhqk", q, k) * (Dh ** -0.5)
        p = _softmax(scores, axis=-1)
        o = torch.einsum("bhqk,bhkd->bhqd", p, v)
        return (lambda x, *d: x.permute(*d))(o, 0, 2, 1, 3).reshape(B, Sq, Hq * Dh) @ wo

    def _B_ref(x, w_gate, w_up, w_down):
        g = torch.einsum("bsd,df->bsf", x, w_gate)
        u = torch.einsum("bsd,df->bsf", x, w_up)
        h = _sigmoid(g) * u
        return torch.einsum("bsf,fd->bsd", h, w_down)

    x = A_x
    h = x + _A_ref(_layernorm(x, axis=-1), A_k_cache, A_v_cache, A_wq, A_wo)
    h2 = h + _B_ref(_layernorm(h, axis=-1), B_w_gate, B_w_up, B_w_down)
    return h2
```

```cpp
#include <hip/hip_runtime.h>
#include <hip/hip_bf16.h>
#include <stdint.h>

typedef unsigned short u16;
typedef unsigned v4u __attribute__((ext_vector_type(4)));
typedef __bf16 bf16x2 __attribute__((ext_vector_type(2)));

constexpr int B = 2, D = 512, HQ = 32, HKV = 8, SK = 32768, DH = 128, W = 4096, F = 1792;
constexpr int NSPLIT = 16, KPS = W / NSPLIT;

__device__ __forceinline__ float bf2f(u16 x) { return __uint_as_float(((unsigned)x) << 16); }
__device__ __forceinline__ u16 f2bf(float f) { unsigned u = __float_as_uint(f); return (u16)((u + 0x7fffu + ((u >> 16) & 1u)) >> 16); }
__device__ __forceinline__ float rbf(float f) { return bf2f(f2bf(f)); }
__device__ __forceinline__ float lo(unsigned w) { return __uint_as_float(w << 16); }
__device__ __forceinline__ float hi(unsigned w) { return __uint_as_float(w & 0xffff0000u); }

__device__ __forceinline__ float wave_sum(float v) {
#pragma unroll
  for (int o = 32; o > 0; o >>= 1) v += __shfl_xor(v, o);
  return v;
}
__device__ __forceinline__ float wave_max(float v) {
#pragma unroll
  for (int o = 32; o > 0; o >>= 1) v = fmaxf(v, __shfl_xor(v, o));
  return v;
}

__device__ void ln_row(const float* xin, float* out, int lane) {
  float v[8]; float s = 0.f;
#pragma unroll
  for (int i = 0; i < 8; ++i) { v[i] = xin[lane + 64 * i]; s += v[i]; }
  s = wave_sum(s);
  float mean = s * (1.0f / D);
  float q = 0.f;
#pragma unroll
  for (int i = 0; i < 8; ++i) { float d = v[i] - mean; q += d * d; }
  q = wave_sum(q);
  float var = rbf(q * (1.0f / D));
  float mb = rbf(mean);
  float den = rbf(sqrtf(rbf(var + 1e-5f)));
#pragma unroll
  for (int i = 0; i < 8; ++i) out[lane + 64 * i] = rbf(rbf(v[i] - mb) / den);
}

__global__ void __launch_bounds__(256) k_qproj(const u16* __restrict__ x, const u16* __restrict__ wq, float* __restrict__ qout) {
  __shared__ float xs[B][D], xn[B][D];
  __shared__ float red[4][B][16];
  int tid = threadIdx.x, lane = tid & 63, wave = tid >> 6;
  int cbk = (blockIdx.x % 8) * 32 + blockIdx.x / 8;
  for (int i = tid; i < B * D; i += 256) xs[i / D][i % D] = bf2f(x[i]);
  v4u w[4];
#pragma unroll
  for (int i = 0; i < 4; ++i) w[i] = *(const v4u*)(wq + (size_t)((tid >> 1) + 128 * i) * (HQ * DH) + cbk * 16 + (tid & 1) * 8);
  __syncthreads();
  if (wave < B) ln_row(xs[wave], xn[wave], lane);
  __syncthreads();
  float acc[B][8];
#pragma unroll
  for (int b = 0; b < B; ++b)
#pragma unroll
    for (int k = 0; k < 8; ++k) acc[b][k] = 0.f;
#pragma unroll
  for (int i = 0; i < 4; ++i) {
    int r = (tid >> 1) + 128 * i;
#pragma unroll
    for (int b = 0; b < B; ++b) {
      float xv = xn[b][r];
#pragma unroll
      for (int k = 0; k < 4; ++k) { acc[b][2 * k] += xv * lo(w[i][k]); acc[b][2 * k + 1] += xv * hi(w[i][k]); }
    }
  }
#pragma unroll
  for (int b = 0; b < B; ++b)
#pragma unroll
    for (int k = 0; k < 8; ++k) {
      float v = acc[b][k];
#pragma unroll
      for (int o = 32; o > 1; o >>= 1) v += __shfl_xor(v, o);
      if (lane < 2) red[wave][b][lane * 8 + k] = v;
    }
  __syncthreads();
  if (tid < 32) {
    int b = tid >> 4, c = tid & 15;
    float v = red[0][b][c] + red[1][b][c] + red[2][b][c] + red[3][b][c];
    qout[b * (HQ * DH) + cbk * 16 + c] = rbf(v);
  }
}

__global__ void __launch_bounds__(256) k_attn(const u16* __restrict__ kc, const u16* __restrict__ vc, const float* __restrict__ q,
                                              float* __restrict__ pml, float* __restrict__ po) {
  __shared__ float ps[4][4][64];
  __shared__ float mrg[4][4][DH + 2];
  int tid = threadIdx.x, lane = tid & 63, wave = tid >> 6, r = lane >> 4, c = lane & 15;
  int kvh = blockIdx.x % 8, rest = blockIdx.x / 8, b = rest >> 4, s = rest & 15;
  size_t base = ((size_t)(b * HKV + kvh) * SK + (SK - W) + s * KPS + wave * 64) * DH;
  v4u K[16], V[16];
#pragma unroll
  for (int i = 0; i < 16; ++i) K[i] = __builtin_nontemporal_load((const v4u*)(kc + base + (size_t)(r + 4 * i) * DH + 8 * c));
#pragma unroll
  for (int i = 0; i < 16; ++i) V[i] = __builtin_nontemporal_load((const v4u*)(vc + base + (size_t)(r + 4 * i) * DH + 8 * c));
  float qf[4][8];
#pragma unroll
  for (int g = 0; g < 4; ++g)
#pragma unroll
    for (int k = 0; k < 8; ++k) qf[g][k] = q[(size_t)b * (HQ * DH) + (kvh * 4 + g) * DH + 8 * c + k];
#pragma unroll
  for (int i = 0; i < 16; ++i) {
#pragma unroll
    for (int g = 0; g < 4; ++g) {
      float d = 0.f;
#pragma unroll
      for (int k = 0; k < 4; ++k) { d += qf[g][2 * k] * lo(K[i][k]); d += qf[g][2 * k + 1] * hi(K[i][k]); }
#pragma unroll
      for (int o = 8; o > 0; o >>= 1) d += __shfl_xor(d, o);
      if (c == 0) ps[wave][g][r + 4 * i] = rbf(rbf(d) * 0.08838834764831845f);
    }
  }
  __builtin_amdgcn_wave_barrier();
  asm volatile("s_waitcnt lgkmcnt(0)" ::: "memory");
  float m[4], l[4];
#pragma unroll
  for (int g = 0; g < 4; ++g) {
    float sv = ps[wave][g][lane];
    float mx = wave_max(sv);
    float p = __expf(sv - mx);
    l[g] = wave_sum(p);
    m[g] = mx;
    ps[wave][g][lane] = p;
  }
  __builtin_amdgcn_wave_barrier();
  asm volatile("s_waitcnt lgkmcnt(0)" ::: "memory");
  float acc[4][8];
#pragma unroll
  for (int g = 0; g < 4; ++g)
#pragma unroll
    for (int k = 0; k < 8; ++k) acc[g][k] = 0.f;
#pragma unroll
  for (int i = 0; i < 16; ++i) {
#pragma unroll
    for (int g = 0; g < 4; ++g) {
      float p = ps[wave][g][r + 4 * i];
#pragma unroll
      for (int k = 0; k < 4; ++k) { acc[g][2 * k] += p * lo(V[i][k]); acc[g][2 * k + 1] += p * hi(V[i][k]); }
    }
  }
#pragma unroll
  for (int g = 0; g < 4; ++g)
#pragma unroll
    for (int k = 0; k < 8; ++k) { float v = acc[g][k]; v += __shfl_xor(v, 16); v += __shfl_xor(v, 32); acc[g][k] = v; }
  if (r == 0) {
#pragma unroll
    for (int g = 0; g < 4; ++g) {
#pragma unroll
      for (int k = 0; k < 8; ++k) mrg[wave][g][8 * c + k] = acc[g][k];
      if (c == 0) { mrg[wave][g][DH] = m[g]; mrg[wave][g][DH + 1] = l[g]; }
    }
  }
  __syncthreads();
  {
    int g = wave;
    float M = fmaxf(fmaxf(mrg[0][g][DH], mrg[1][g][DH]), fmaxf(mrg[2][g][DH], mrg[3][g][DH]));
    float L = 0.f, o0 = 0.f, o1 = 0.f;
#pragma unroll
    for (int w = 0; w < 4; ++w) {
      float wt = __expf(mrg[w][g][DH] - M);
      L += wt * mrg[w][g][DH + 1];
      o0 += wt * mrg[w][g][2 * lane]; o1 += wt * mrg[w][g][2 * lane + 1];
    }
    size_t slot = ((size_t)(b * HQ + kvh * 4 + g) * NSPLIT + s);
    po[slot * DH + 2 * lane] = o0; po[slot * DH + 2 * lane + 1] = o1;
    if (lane == 0) { pml[slot * 2] = M; pml[slot * 2 + 1] = L; }
  }
}

__global__ void __launch_bounds__(256) k_oproj(const float* __restrict__ pml, const float* __restrict__ po, const u16* __restrict__ wo,
                                               float* __restrict__ hpart) {
  __shared__ float os[B][512];
  __shared__ float red[4][B][16];
  int tid = threadIdx.x, lane = tid & 63, wave = tid >> 6;
  int kvh = blockIdx.x % 8, cb = blockIdx.x / 8;
  v4u w[4];
#pragma unroll
  for (int i = 0; i < 4; ++i) w[i] = *(const v4u*)(wo + (size_t)(kvh * 512 + (tid >> 1) + 128 * i) * D + cb * 16 + (tid & 1) * 8);
  for (int i = tid; i < B * 512; i += 256) {
    int b = i >> 9, j = i & 511, g = j >> 7, d = j & 127;
    size_t hb = (size_t)(b * HQ + kvh * 4 + g) * NSPLIT;
    float M = -INFINITY;
    for (int s = 0; s < NSPLIT; ++s) M = fmaxf(M, pml[(hb + s) * 2]);
    float L = 0.f, o = 0.f;
    for (int s = 0; s < NSPLIT; ++s) { float wt = __expf(pml[(hb + s) * 2] - M); L += wt * pml[(hb + s) * 2 + 1]; o += wt * po[(hb + s) * DH + d]; }
    os[b][j] = rbf(o / L);
  }
  __syncthreads();
  float acc[B][8];
#pragma unroll
  for (int b = 0; b < B; ++b)
#pragma unroll
    for (int k = 0; k < 8; ++k) acc[b][k] = 0.f;
#pragma unroll
  for (int i = 0; i < 4; ++i) {
    int r = (tid >> 1) + 128 * i;
#pragma unroll
    for (int b = 0; b < B; ++b) {
      float xv = os[b][r];
#pragma unroll
      for (int k = 0; k < 4; ++k) { acc[b][2 * k] += xv * lo(w[i][k]); acc[b][2 * k + 1] += xv * hi(w[i][k]); }
    }
  }
#pragma unroll
  for (int b = 0; b < B; ++b)
#pragma unroll
    for (int k = 0; k < 8; ++k) {
      float v = acc[b][k];
#pragma unroll
      for (int o = 32; o > 1; o >>= 1) v += __shfl_xor(v, o);
      if (lane < 2) red[wave][b][lane * 8 + k] = v;
    }
  __syncthreads();
  if (tid < 32) {
    int b = tid >> 4, c = tid & 15;
    float v = red[0][b][c] + red[1][b][c] + red[2][b][c] + red[3][b][c];
    hpart[(size_t)(kvh * B + b) * D + cb * 16 + c] = v;
  }
}

__global__ void __launch_bounds__(256) k_gateup(const u16* __restrict__ x, const float* __restrict__ hpart, const u16* __restrict__ wg,
                                                const u16* __restrict__ wu, float* __restrict__ hout, float* __restrict__ hact) {
  __shared__ float hs[B][D], hn[B][D];
  __shared__ float red[4][B][32];
  int tid = threadIdx.x, lane = tid & 63, wave = tid >> 6;
  int fb = blockIdx.x;
  v4u w[4], u[4];
#pragma unroll
  for (int i = 0; i < 4; ++i) {
    w[i] = *(const v4u*)(wg + (size_t)((tid >> 1) + 128 * i) * F + fb * 16 + (tid & 1) * 8);
    u[i] = *(const v4u*)(wu + (size_t)((tid >> 1) + 128 * i) * F + fb * 16 + (tid & 1) * 8);
  }
  for (int i = tid; i < B * D; i += 256) {
    float s = 0.f;
#pragma unroll
    for (int k = 0; k < 8; ++k) s += hpart[(size_t)k * B * D + i];
    float h = rbf(bf2f(x[i]) + rbf(s));
    hs[i / D][i % D] = h;
    if (fb == 0) hout[i] = h;
  }
  __syncthreads();
  if (wave < B) ln_row(hs[wave], hn[wave], lane);
  __syncthreads();
  float ag[B][8], au[B][8];
#pragma unroll
  for (int b = 0; b < B; ++b)
#pragma unroll
    for (int k = 0; k < 8; ++k) { ag[b][k] = 0.f; au[b][k] = 0.f; }
#pragma unroll
  for (int i = 0; i < 4; ++i) {
    int r = (tid >> 1) + 128 * i;
#pragma unroll
    for (int b = 0; b < B; ++b) {
      float xv = hn[b][r];
#pragma unroll
      for (int k = 0; k < 4; ++k) {
        ag[b][2 * k] += xv * lo(w[i][k]); ag[b][2 * k + 1] += xv * hi(w[i][k]);
        au[b][2 * k] += xv * lo(u[i][k]); au[b][2 * k + 1] += xv * hi(u[i][k]);
      }
    }
  }
#pragma unroll
  for (int b = 0; b < B; ++b)
#pragma unroll
    for (int k = 0; k < 8; ++k) {
      float v = ag[b][k], v2 = au[b][k];
#pragma unroll
      for (int o = 32; o > 1; o >>= 1) { v += __shfl_xor(v, o); v2 += __shfl_xor(v2, o); }
      if (lane < 2) { red[wave][b][lane * 8 + k] = v; red[wave][b][16 + lane * 8 + k] = v2; }
    }
  __syncthreads();
  if (tid < 32) {
    int b = tid >> 4, c = tid & 15;
    float g = rbf(red[0][b][c] + red[1][b][c] + red[2][b][c] + red[3][b][c]);
    float uu = rbf(red[0][b][16 + c] + red[1][b][16 + c] + red[2][b][16 + c] + red[3][b][16 + c]);
    float sg = rbf(1.0f / (1.0f + __expf(-g)));
    hact[b * F + fb * 16 + c] = rbf(sg * uu);
  }
}

__global__ void __launch_bounds__(256) k_down(const float* __restrict__ h, const float* __restrict__ hact, const u16* __restrict__ wd, u16* __restrict__ out) {
  __shared__ float as[B][F];
  __shared__ float red[4][B][16];
  int tid = threadIdx.x, lane = tid & 63, wave = tid >> 6;
  int cb = blockIdx.x;
  v4u w[14];
#pragma unroll
  for (int i = 0; i < 14; ++i) w[i] = *(const v4u*)(wd + (size_t)((tid >> 1) + 128 * i) * D + cb * 16 + (tid & 1) * 8);
  for (int i = tid; i < B * F; i += 256) as[i / F][i % F] = hact[i];
  __syncthreads();
  float acc[B][8];
#pragma unroll
  for (int b = 0; b < B; ++b)
#pragma unroll
    for (int k = 0; k < 8; ++k) acc[b][k] = 0.f;
#pragma unroll
  for (int i = 0; i < 14; ++i) {
    int r = (tid >> 1) + 128 * i;
#pragma unroll
    for (int b = 0; b < B; ++b) {
      float xv = as[b][r];
#pragma unroll
      for (int k = 0; k < 4; ++k) { acc[b][2 * k] += xv * lo(w[i][k]); acc[b][2 * k + 1] += xv * hi(w[i][k]); }
    }
  }
#pragma unroll
  for (int b = 0; b < B; ++b)
#pragma unroll
    for (int k = 0; k < 8; ++k) {
      float v = acc[b][k];
#pragma unroll
      for (int o = 32; o > 1; o >>= 1) v += __shfl_xor(v, o);
      if (lane < 2) red[wave][b][lane * 8 + k] = v;
    }
  __syncthreads();
  if (tid < 32) {
    int b = tid >> 4, c = tid & 15;
    float v = rbf(red[0][b][c] + red[1][b][c] + red[2][b][c] + red[3][b][c]);
    int col = cb * 16 + c;
    out[b * D + col] = f2bf(h[b * D + col] + v);
  }
}

extern "C" void kernel_launch(void* const* d_in, const int* in_sizes, int n_in, void* d_out, int out_size, void* d_ws, size_t ws_size,
                              hipStream_t stream) {
  const u16* x = (const u16*)d_in[0];
  const u16* kc = (const u16*)d_in[1];
  const u16* vc = (const u16*)d_in[2];
  const u16* wq = (const u16*)d_in[3];
  const u16* wo = (const u16*)d_in[4];
  const u16* wg = (const u16*)d_in[6];
  const u16* wu = (const u16*)d_in[7];
  const u16* wd = (const u16*)d_in[8];
  float* ws = (float*)d_ws;
  float* q = ws;
  float* pml = q + 8192;
  float* po = pml + 2048;
  float* hpart = po + 131072;
  float* h = hpart + 8192;
  float* hact = h + 1024;
  k_qproj<<<256, 256, 0, stream>>>(x, wq, q);
  k_attn<<<256, 256, 0, stream>>>(kc, vc, q, pml, po);
  k_oproj<<<256, 256, 0, stream>>>(pml, po, wo, hpart);
  k_gateup<<<112, 256, 0, stream>>>(x, hpart, wg, wu, h, hact);
  k_down<<<32, 256, 0, stream>>>(h, hact, wd, (u16*)d_out);
}
```

```cpp
#include <hip/hip_runtime.h>
#include <stdint.h>

typedef unsigned short u16;
typedef unsigned long long u64;
typedef unsigned v4u __attribute__((ext_vector_type(4)));
typedef float f32x4 __attribute__((ext_vector_type(4)));
typedef short bf16x8 __attribute__((ext_vector_type(8)));
typedef unsigned short u16x4 __attribute__((ext_vector_type(4)));
#define GAS __attribute__((address_space(1)))
#define LAS __attribute__((address_space(3)))
typedef GAS u64 gu64;
typedef GAS unsigned gu32;
#define RLX_AGENT __ATOMIC_RELAXED, __HIP_MEMORY_SCOPE_AGENT

constexpr int B = 2, D = 512, HQ = 32, HKV = 8, SK = 32768, DH = 128, W = 4096, F = 1792;
constexpr int NW = 8;
constexpr int NT = (NW + 1) * 64;
constexpr unsigned SPIN_CAP = 400000;

constexpr size_t WS_EPOCH = 0;
constexpr size_t WS_GQ = 4096;
constexpr size_t WS_GP = WS_GQ + 4096 * 8;
constexpr size_t WS_GS = WS_GP + 65536 * 8;
constexpr size_t WS_GH = WS_GS + 2048 * 8;
constexpr size_t WS_GA = WS_GH + 8192 * 8;
constexpr size_t WS_STAMP = 4u << 20;

constexpr int VROW = 288;
constexpr int L_V = 0;
constexpr int L_XS = L_V + NW * 32 * VROW;
constexpr int L_XN = L_XS + 4096;
constexpr int L_HS = L_XN + 4096;
constexpr int L_RED = L_HS + 4096;
constexpr int L_QS = L_RED + 4096;
constexpr int L_PS = L_QS + 5 * 256;
constexpr int L_OP = L_PS + NW * 320;
constexpr int L_ML = L_OP + NW * 4 * 128 * 4;
constexpr int L_WT = L_ML + NW * 4 * 2 * 4;
constexpr int L_ACT = L_WT + 2 * 4 * 16 * 4;
constexpr int L_TAG = L_ACT + 2 * F * 4;
constexpr int L_END = L_TAG + 16;

__device__ __forceinline__ float bf2f(unsigned x) { return __uint_as_float(x << 16); }
__device__ __forceinline__ unsigned f2bf(float f) { unsigned u = __float_as_uint(f); return (u + 0x7fffu + ((u >> 16) & 1u)) >> 16; }
__device__ __forceinline__ float rbf(float f) { return bf2f(f2bf(f)); }
__device__ __forceinline__ float lo(unsigned w) { return __uint_as_float(w << 16); }
__device__ __forceinline__ float hi(unsigned w) { return __uint_as_float(w & 0xffff0000u); }

template <int CTRL> __device__ __forceinline__ float dpp(float x) {
  return __builtin_bit_cast(float, __builtin_amdgcn_mov_dpp(__builtin_bit_cast(int, x), CTRL, 0xf, 0xf, true));
}
constexpr int XOR1 = 0xB1, XOR2 = 0x4E, ROR4 = 0x124, ROR8 = 0x128;
__device__ __forceinline__ float x16_sum(float x) {
  auto s = __builtin_amdgcn_permlane16_swap(__float_as_uint(x), __float_as_uint(x), false, false);
  return __uint_as_float(s[0]) + __uint_as_float(s[1]);
}
__device__ __forceinline__ float x32_sum(float x) {
  auto t = __builtin_amdgcn_permlane32_swap(__float_as_uint(x), __float_as_uint(x), false, false);
  return __uint_as_float(t[0]) + __uint_as_float(t[1]);
}
__device__ __forceinline__ float x16_max(float x) {
  auto s = __builtin_amdgcn_permlane16_swap(__float_as_uint(x), __float_as_uint(x), false, false);
  return fmaxf(__uint_as_float(s[0]), __uint_as_float(s[1]));
}
__device__ __forceinline__ float row_sum(float v) { v += dpp<XOR1>(v); v += dpp<XOR2>(v); v += dpp<ROR4>(v); v += dpp<ROR8>(v); return v; }
__device__ __forceinline__ float row_max(float v) { v = fmaxf(v, dpp<XOR1>(v)); v = fmaxf(v, dpp<XOR2>(v)); v = fmaxf(v, dpp<ROR4>(v)); v = fmaxf(v, dpp<ROR8>(v)); return v; }
__device__ __forceinline__ float wave_sum(float v) { return x32_sum(x16_sum(row_sum(v))); }
__device__ __forceinline__ float par_sum(float v) { v += dpp<XOR2>(v); v += dpp<ROR4>(v); v += dpp<ROR8>(v); return x32_sum(x16_sum(v)); }
__device__ __forceinline__ float oct_sum(float v) { v += dpp<ROR8>(v); return x32_sum(x16_sum(v)); }

#define BAR() do { asm volatile("s_waitcnt lgkmcnt(0)" ::: "memory"); __builtin_amdgcn_s_barrier(); asm volatile("" ::: "memory"); } while (0)
#define STAMP(i) do { } while (0)

__device__ __forceinline__ v4u ldnt(const u16* p) { return __builtin_nontemporal_load((const v4u*)p); }
__device__ __forceinline__ void st_gran(gu64* g, unsigned tag, unsigned v) { __hip_atomic_store(g, ((u64)tag << 32) | v, RLX_AGENT); }

template <int N> __device__ __forceinline__ void sweep(gu64* g, int S, unsigned tag, unsigned (&v)[N], bool active) {
  for (unsigned spins = 0;; ++spins) {
    bool ok = true;
    u64 x[N];
#pragma unroll
    for (int k = 0; k < N; ++k) x[k] = active ? __hip_atomic_load(g + (size_t)k * S, RLX_AGENT) : ((u64)tag << 32);
#pragma unroll
    for (int k = 0; k < N; ++k) { v[k] = (unsigned)x[k]; ok &= (unsigned)(x[k] >> 32) == tag; }
    if (__all(ok) || spins > SPIN_CAP) return;
  }
}

__device__ __forceinline__ void ln8(float (&v)[8]) {
  float s = 0.f;
#pragma unroll
  for (int i = 0; i < 8; ++i) s += v[i];
  s = wave_sum(s);
  const float mean = s * (1.0f / D);
  float q = 0.f;
#pragma unroll
  for (int i = 0; i < 8; ++i) { const float d = v[i] - mean; q += d * d; }
  q = wave_sum(q);
  const float var = rbf(q * (1.0f / D));
  const float mb = rbf(mean);
  const float den = rbf(sqrtf(rbf(var + 1e-5f)));
#pragma unroll
  for (int i = 0; i < 8; ++i) v[i] = rbf(rbf(v[i] - mb) / den);
}

__global__ void __launch_bounds__(NT, 3) mega(const u16* __restrict__ x, const u16* __restrict__ kc, const u16* __restrict__ vc,
                                              const u16* __restrict__ wq, const u16* __restrict__ wo, const u16* __restrict__ wg,
                                              const u16* __restrict__ wu, const u16* __restrict__ wd, u16* __restrict__ out, unsigned char* ws_) {
  extern __shared__ __attribute__((aligned(16))) unsigned char smem[];
  GAS unsigned char* ws = (GAS unsigned char*)ws_;
  const int tid = threadIdx.x, lane = tid & 63, wave = __builtin_amdgcn_readfirstlane(tid >> 6);
  const int xc = blockIdx.x & 7, r = blockIdx.x >> 3;
  const int kvh = xc, b = r >> 4, s = r & 15;
  const int cbk = kvh * 32 + r;
  const int role = r < 8 ? 0 : r < 22 ? 1 : r < 26 ? 2 : 3;
  float* XS = (float*)(smem + L_XS); float* XN = (float*)(smem + L_XN); float* HS = (float*)(smem + L_HS);
  float* RED = (float*)(smem + L_RED); float* OP = (float*)(smem + L_OP); float* ML = (float*)(smem + L_ML);
  float* WT = (float*)(smem + L_WT); float* ACT = (float*)(smem + L_ACT);
  gu64* GQ = (gu64*)(ws + WS_GQ); gu64* GP = (gu64*)(ws + WS_GP); gu64* GS = (gu64*)(ws + WS_GS);
  gu64* GH = (gu64*)(ws + WS_GH); gu64* GA = (gu64*)(ws + WS_GA);
  unsigned* TAGW = (unsigned*)(smem + L_TAG);

  if (wave == NW) {
    STAMP(0);
    const unsigned tag = __hip_atomic_load((gu32*)(ws + WS_EPOCH), RLX_AGENT) + 1u;
    {
      v4u xa = *(const v4u*)(x + 8 * lane), xb = *(const v4u*)(x + D + 8 * lane);
      float va[8], vb[8];
#pragma unroll
      for (int k = 0; k < 4; ++k) { va[2 * k] = lo(xa[k]); va[2 * k + 1] = hi(xa[k]); vb[2 * k] = lo(xb[k]); vb[2 * k + 1] = hi(xb[k]); }
#pragma unroll
      for (int k = 0; k < 8; ++k) { XS[8 * lane + k] = va[k]; XS[D + 8 * lane + k] = vb[k]; }
      ln8(va); ln8(vb);
#pragma unroll
      for (int k = 0; k < 8; ++k) { XN[8 * lane + k] = va[k]; XN[D + 8 * lane + k] = vb[k]; }
      if (lane == 0) TAGW[0] = tag;
      ((unsigned*)(smem + L_QS + 4 * 256))[lane] = 0u;
      if (lane < 16) {
#pragma unroll
        for (int w = 0; w < NW; ++w) ((unsigned*)(smem + L_PS + w * 320 + 4 * 64))[lane] = 0u;
      }
    }
    BAR();
    BAR();
    if (lane < 16) {
      float a0 = 0.f, a1 = 0.f;
#pragma unroll
      for (int w = 0; w < NW; ++w) { a0 += RED[w * 128 + lane]; a1 += RED[w * 128 + 16 + lane]; }
      st_gran(GQ + cbk * 16 + lane, tag, f2bf(a0) | (f2bf(a1) << 16));
    }
    STAMP(1);
    {
      unsigned v[8];
      sweep<8>(GQ + kvh * 512 + lane, 64, tag, v, true);
      u16* QS = (u16*)(smem + L_QS);
#pragma unroll
      for (int k = 0; k < 8; ++k) QS[k * 64 + lane] = (u16)(b ? (v[k] >> 16) : (v[k] & 0xffffu));
    }
    STAMP(2);
    BAR();
    BAR();
    if (role == 0) {
      const int ko = xc;
      unsigned v[4];
      const int bb = lane >> 5, ss = (lane & 31) >> 1, hp = lane & 1;
      sweep<4>(GS + ((bb * 8 + ko) * 16 + ss) * 8 + hp * 4, 1, tag, v, true);
#pragma unroll
      for (int j = 0; j < 2; ++j) {
        const float m = __uint_as_float(v[2 * j]), l = __uint_as_float(v[2 * j + 1]);
        float M = m; M = fmaxf(M, dpp<XOR2>(M)); M = fmaxf(M, dpp<ROR4>(M)); M = fmaxf(M, dpp<ROR8>(M)); M = x16_max(M);
        const float wgt = l * __expf(m - M);
        float L = wgt; L += dpp<XOR2>(L); L += dpp<ROR4>(L); L += dpp<ROR8>(L); L = x16_sum(L);
        WT[(bb * 4 + hp * 2 + j) * 16 + ss] = wgt / L;
      }
      BAR();
    }
    return;
  }

  const int li = lane & 15, lg = lane >> 4;
  v4u wqr[2], K[8], V[8], rw[8];
#pragma unroll
  for (int i = 0; i < 2; ++i) wqr[i] = ldnt(wq + (size_t)((tid >> 1) + 256 * i) * (HQ * DH) + cbk * 16 + (tid & 1) * 8);
  {
    const size_t kvbase = ((size_t)(b * HKV + kvh) * SK + (SK - W) + s * 256 + wave * 32) * DH;
#pragma unroll
    for (int t = 0; t < 2; ++t)
#pragma unroll
      for (int kk = 0; kk < 4; ++kk) K[t * 4 + kk] = ldnt(kc + kvbase + (size_t)(t * 16 + li) * DH + kk * 32 + lg * 8);
#pragma unroll
    for (int j = 0; j < 8; ++j) V[j] = ldnt(vc + kvbase + (size_t)(4 * j + lg) * DH + li * 8);
  }
  {
    const int fb = xc * 14 + (r - 8), cb = xc * 4 + (r - 22);
    const size_t o0 = (size_t)(xc * 512 + (tid >> 3)) * D + r * 64 + (tid & 7) * 8;
    const size_t o1 = (size_t)(tid >> 1) * F + fb * 16 + (tid & 1) * 8;
    const size_t o2 = (size_t)(tid >> 1) * D + cb * 16 + (tid & 1) * 8;
#pragma unroll
    for (int i = 0; i < 8; ++i) {
      const u16* p = x;
      if (role == 0) p = wo + o0 + (size_t)64 * i * D;
      if (role == 1 && i < 4) p = (i < 2 ? wg : wu) + o1 + (size_t)256 * (i & 1) * F;
      if (role == 2 && i < 7) p = wd + o2 + (size_t)256 * i * D;
      rw[i] = ldnt(p);
    }
  }
  BAR();
  const unsigned tag = __builtin_amdgcn_readfirstlane(TAGW[0]);
  {
    float acc[2][8];
#pragma unroll
    for (int bb = 0; bb < 2; ++bb)
#pragma unroll
      for (int k = 0; k < 8; ++k) acc[bb][k] = 0.f;
#pragma unroll
    for (int i = 0; i < 2; ++i) {
      const int row = (tid >> 1) + 256 * i;
#pragma unroll
      for (int bb = 0; bb < 2; ++bb) {
        const float xv = XN[bb * D + row];
#pragma unroll
        for (int k = 0; k < 4; ++k) { acc[bb][2 * k] += xv * lo(wqr[i][k]); acc[bb][2 * k + 1] += xv * hi(wqr[i][k]); }
      }
    }
#pragma unroll
    for (int bb = 0; bb < 2; ++bb)
#pragma unroll
      for (int k = 0; k < 8; ++k) { const float v = par_sum(acc[bb][k]); if (lane < 2) RED[wave * 128 + bb * 16 + lane * 8 + k] = v; }
  }
  BAR();
  BAR();
  {
    f32x4 c[2];
    bf16x8 qa[4];
#pragma unroll
    for (int kk = 0; kk < 4; ++kk) qa[kk] = *(const bf16x8*)(smem + L_QS + (li < 4 ? li : 4) * 256 + kk * 64 + lg * 16);
#pragma unroll
    for (int t = 0; t < 2; ++t) {
      c[t] = f32x4{0.f, 0.f, 0.f, 0.f};
#pragma unroll
      for (int kk = 0; kk < 4; ++kk) c[t] = __builtin_amdgcn_mfma_f32_16x16x32_bf16(qa[kk], __builtin_bit_cast(bf16x8, K[t * 4 + kk]), c[t], 0, 0, 0);
    }
    unsigned char* vb = smem + L_V + wave * 32 * VROW;
#pragma unroll
    for (int j = 0; j < 8; ++j) *(v4u*)(vb + (4 * j + lg) * VROW + li * 16) = V[j];
    u16* PS = (u16*)(smem + L_PS + wave * 320);
#pragma unroll
    for (int h = 0; h < 4; ++h) {
      const float s0 = rbf(rbf(c[0][h]) * 0.08838834764831845f), s1 = rbf(rbf(c[1][h]) * 0.08838834764831845f);
      const float mx = row_max(fmaxf(s0, s1));
      const unsigned p0 = f2bf(__expf(s0 - mx)), p1 = f2bf(__expf(s1 - mx));
      const float l = row_sum(bf2f(p0) + bf2f(p1));
      if (lg == 0) { PS[h * 32 + li] = (u16)p0; PS[h * 32 + 16 + li] = (u16)p1; }
      if (lane == 0) { ML[(wave * 4 + h) * 2] = mx; ML[(wave * 4 + h) * 2 + 1] = l; }
    }
    asm volatile("s_waitcnt lgkmcnt(0)" ::: "memory");
    const bf16x8 pa = *(const bf16x8*)(smem + L_PS + wave * 320 + (li < 4 ? li : 4) * 64 + lg * 16);
    const unsigned va = (unsigned)(size_t)(LAS unsigned char*)(vb) + (8 * lg + (li >> 2)) * VROW + 8 * (li & 3);
#pragma unroll
    for (int cq = 0; cq < 2; ++cq) {
      u16x4 t[8];
      asm volatile(
          "ds_read_b64_tr_b16 %0, %8 offset:%c9\n\t"
          "ds_read_b64_tr_b16 %1, %8 offset:%c10\n\t"
          "ds_read_b64_tr_b16 %2, %8 offset:%c11\n\t"
          "ds_read_b64_tr_b16 %3, %8 offset:%c12\n\t"
          "ds_read_b64_tr_b16 %4, %8 offset:%c13\n\t"
          "ds_read_b64_tr_b16 %5, %8 offset:%c14\n\t"
          "ds_read_b64_tr_b16 %6, %8 offset:%c15\n\t"
          "ds_read_b64_tr_b16 %7, %8 offset:%c16\n\t"
          "s_waitcnt lgkmcnt(0)"
          : "=&v"(t[0]), "=&v"(t[1]), "=&v"(t[2]), "=&v"(t[3]), "=&v"(t[4]), "=&v"(t[5]), "=&v"(t[6]), "=&v"(t[7])
          : "v"(va), "i"((cq * 4 + 0) * 32), "i"((cq * 4 + 0) * 32 + 4 * VROW), "i"((cq * 4 + 1) * 32), "i"((cq * 4 + 1) * 32 + 4 * VROW),
            "i"((cq * 4 + 2) * 32), "i"((cq * 4 + 2) * 32 + 4 * VROW), "i"((cq * 4 + 3) * 32), "i"((cq * 4 + 3) * 32 + 4 * VROW)
          : "memory");
#pragma unroll
      for (int cc = 0; cc < 4; ++cc) {
        bf16x8 vbf;
#pragma unroll
        for (int e = 0; e < 4; ++e) { vbf[e] = (short)t[2 * cc][e]; vbf[4 + e] = (short)t[2 * cc + 1][e]; }
        f32x4 o = __builtin_amdgcn_mfma_f32_16x16x32_bf16(pa, vbf, f32x4{0.f, 0.f, 0.f, 0.f}, 0, 0, 0);
        if (lg == 0) {
#pragma unroll
          for (int h = 0; h < 4; ++h) OP[(wave * 4 + h) * 128 + (cq * 4 + cc) * 16 + li] = o[h];
        }
      }
    }
  }
  BAR();
  if (wave == 0) STAMP(3);
  if (tid < 256) {
    const int h = tid >> 6, dp = tid & 63;
    float M = -INFINITY;
#pragma unroll
    for (int w = 0; w < NW; ++w) M = fmaxf(M, ML[(w * 4 + h) * 2]);
    float L = 0.f, o0 = 0.f, o1 = 0.f;
#pragma unroll
    for (int w = 0; w < NW; ++w) {
      const float wt = __expf(ML[(w * 4 + h) * 2] - M);
      L += wt * ML[(w * 4 + h) * 2 + 1];
      const float2 a = *(const float2*)(OP + (w * 4 + h) * 128 + 2 * dp);
      o0 += wt * a.x; o1 += wt * a.y;
    }
    const float inv = 1.0f / L;
    const int slot = (b * 8 + kvh) * 16 + s;
    st_gran(GP + slot * 256 + tid, tag, f2bf(o0 * inv) | (f2bf(o1 * inv) << 16));
    if (dp == 0) { st_gran(GS + slot * 8 + h * 2, tag, __float_as_uint(M)); st_gran(GS + slot * 8 + h * 2 + 1, tag, __float_as_uint(L)); }
  }
  if (wave == 0) STAMP(4);
  if (role == 3) return;

  if (role == 0) {
    const int bb = tid >> 8, h = (tid >> 6) & 3, dp = lane;
    unsigned v[16];
    sweep<16>(GP + (size_t)((bb * 8 + xc) * 16) * 256 + h * 64 + dp, 256, tag, v, true);
    BAR();
    if (wave == 0) STAMP(5);
    float o0 = 0.f, o1 = 0.f;
#pragma unroll
    for (int ss = 0; ss < 16; ++ss) { const float wt = WT[(bb * 4 + h) * 16 + ss]; o0 += wt * lo(v[ss]); o1 += wt * hi(v[ss]); }
    *(float2*)(XN + bb * D + h * 128 + 2 * dp) = make_float2(rbf(o0), rbf(o1));
    BAR();
    float acc[2][8];
#pragma unroll
    for (int q = 0; q < 2; ++q)
#pragma unroll
      for (int k = 0; k < 8; ++k) acc[q][k] = 0.f;
#pragma unroll
    for (int i = 0; i < 8; ++i) {
      const int row = (tid >> 3) + 64 * i;
#pragma unroll
      for (int q = 0; q < 2; ++q) {
        const float xv = XN[q * D + row];
#pragma unroll
        for (int k = 0; k < 4; ++k) { acc[q][2 * k] += xv * lo(rw[i][k]); acc[q][2 * k + 1] += xv * hi(rw[i][k]); }
      }
    }
#pragma unroll
    for (int q = 0; q < 2; ++q)
#pragma unroll
      for (int k = 0; k < 8; ++k) { const float v2 = oct_sum(acc[q][k]); if (lane < 8) RED[wave * 128 + q * 64 + lane * 8 + k] = v2; }
    BAR();
    if (tid < 128) {
      float a = 0.f;
#pragma unroll
      for (int w = 0; w < NW; ++w) a += RED[w * 128 + tid];
      const int q = tid >> 6, col = tid & 63;
      st_gran(GH + (xc * 2 + q) * 512 + r * 64 + col, tag, __float_as_uint(a));
    }
    if (wave == 0) STAMP(6);
    return;
  }

  if (role == 1) {
    const int fb = xc * 14 + (r - 8);
    unsigned v[16];
    sweep<16>(GH + tid, 512, tag, v, true);
    if (wave == 0) STAMP(5);
    float s0 = 0.f, s1 = 0.f;
#pragma unroll
    for (int k = 0; k < 8; ++k) { s0 += __uint_as_float(v[2 * k]); s1 += __uint_as_float(v[2 * k + 1]); }
    HS[tid] = rbf(XS[tid] + rbf(s0));
    HS[D + tid] = rbf(XS[D + tid] + rbf(s1));
    BAR();
    if (wave < 2) {
      float hv[8];
#pragma unroll
      for (int k = 0; k < 8; ++k) hv[k] = HS[wave * D + 8 * lane + k];
      ln8(hv);
#pragma unroll
      for (int k = 0; k < 8; ++k) XN[wave * D + 8 * lane + k] = hv[k];
    }
    BAR();
    float ag[2][8], au[2][8];
#pragma unroll
    for (int q = 0; q < 2; ++q)
#pragma unroll
      for (int k = 0; k < 8; ++k) { ag[q][k] = 0.f; au[q][k] = 0.f; }
#pragma unroll
    for (int i = 0; i < 2; ++i) {
      const int row = (tid >> 1) + 256 * i;
#pragma unroll
      for (int q = 0; q < 2; ++q) {
        const float xv = XN[q * D + row];
#pragma unroll
        for (int k = 0; k < 4; ++k) {
          ag[q][2 * k] += xv * lo(rw[i][k]); ag[q][2 * k + 1] += xv * hi(rw[i][k]);
          au[q][2 * k] += xv * lo(rw[2 + i][k]); au[q][2 * k + 1] += xv * hi(rw[2 + i][k]);
        }
      }
    }
#pragma unroll
    for (int q = 0; q < 2; ++q)
#pragma unroll
      for (int k = 0; k < 8; ++k) {
        const float g2 = par_sum(ag[q][k]), u2 = par_sum(au[q][k]);
        if (lane < 2) { RED[wave * 128 + q * 16 + lane * 8 + k] = g2; RED[wave * 128 + 32 + q * 16 + lane * 8 + k] = u2; }
      }
    BAR();
    if (tid < 16) {
      unsigned pk = 0;
#pragma unroll
      for (int q = 0; q < 2; ++q) {
        float g = 0.f, u = 0.f;
#pragma unroll
        for (int w = 0; w < NW; ++w) { g += RED[w * 128 + q * 16 + tid]; u += RED[w * 128 + 32 + q * 16 + tid]; }
        g = rbf(g); u = rbf(u);
        const float sg = rbf(1.0f / (1.0f + __expf(-g)));
        pk |= f2bf(sg * u) << (16 * q);
      }
      st_gran(GA + fb * 16 + tid, tag, pk);
    }
    if (wave == 0) STAMP(6);
    return;
  }

  {
    const int cb = xc * 4 + (r - 22);
    float hres = 0.f;
    if (wave == 0) {
      unsigned v[8];
      const int q = (lane >> 4) & 1, c = lane & 15;
      sweep<8>(GH + q * 512 + cb * 16 + c, 1024, tag, v, true);
      float a = 0.f;
#pragma unroll
      for (int k = 0; k < 8; ++k) a += __uint_as_float(v[k]);
      hres = rbf(XS[q * D + cb * 16 + c] + rbf(a));
    }
    if (wave < 4) {
      unsigned v[4];
      sweep<4>(GA + tid, 512, tag, v, true);
#pragma unroll
      for (int j = 0; j < 4; ++j) { const int idx = tid + 512 * j; ACT[idx] = lo(v[j]); ACT[F + idx] = hi(v[j]); }
    } else {
      unsigned v[3];
      sweep<3>(GA + tid, 512, tag, v, true);
#pragma unroll
      for (int j = 0; j < 3; ++j) { const int idx = tid + 512 * j; ACT[idx] = lo(v[j]); ACT[F + idx] = hi(v[j]); }
    }
    if (wave == 0) STAMP(5);
    BAR();
    float acc[2][8];
#pragma unroll
    for (int q = 0; q < 2; ++q)
#pragma unroll
      for (int k = 0; k < 8; ++k) acc[q][k] = 0.f;
#pragma unroll
    for (int i = 0; i < 7; ++i) {
      const int row = (tid >> 1) + 256 * i;
#pragma unroll
      for (int q = 0; q < 2; ++q) {
        const float xv = ACT[q * F + row];
#pragma unroll
        for (int k = 0; k < 4; ++k) { acc[q][2 * k] += xv * lo(rw[i][k]); acc[q][2 * k + 1] += xv * hi(rw[i][k]); }
      }
    }
#pragma unroll
    for (int q = 0; q < 2; ++q)
#pragma unroll
      for (int k = 0; k < 8; ++k) { const float v2 = par_sum(acc[q][k]); if (lane < 2) RED[wave * 128 + q * 16 + lane * 8 + k] = v2; }
    BAR();
    if (tid < 32) {
      float a = 0.f;
#pragma unroll
      for (int w = 0; w < NW; ++w) a += RED[w * 128 + tid];
      const int q = tid >> 4, c = tid & 15;
      out[q * D + cb * 16 + c] = (u16)f2bf(hres + rbf(a));
      if (cb == 0 && tid == 0) __hip_atomic_store((gu32*)(ws + WS_EPOCH), tag, RLX_AGENT);
    }
    if (wave == 0) STAMP(6);
  }
}

extern "C" void kernel_launch(void* const* d_in, const int* in_sizes, int n_in, void* d_out, int out_size, void* d_ws, size_t ws_size,
                              hipStream_t stream) {
  (void)hipFuncSetAttribute((const void*)mega, hipFuncAttributeMaxDynamicSharedMemorySize, L_END);
  mega<<<256, NT, L_END, stream>>>((const u16*)d_in[0], (const u16*)d_in[1], (const u16*)d_in[2], (const u16*)d_in[3], (const u16*)d_in[4],
                                   (const u16*)d_in[6], (const u16*)d_in[7], (const u16*)d_in[8], (u16*)d_out, (unsigned char*)d_ws);
}
```

```cpp
#include <hip/hip_runtime.h>
#include <stdint.h>

typedef unsigned short u16;
typedef unsigned long long u64;
typedef unsigned v4u __attribute__((ext_vector_type(4)));
typedef unsigned v2u __attribute__((ext_vector_type(2)));
typedef float f32x4 __attribute__((ext_vector_type(4)));
typedef short bf16x8 __attribute__((ext_vector_type(8)));
#define GAS __attribute__((address_space(1)))
#define LAS __attribute__((address_space(3)))
typedef GAS u64 gu64;
typedef GAS unsigned gu32;
#define RLX_AGENT __ATOMIC_RELAXED, __HIP_MEMORY_SCOPE_AGENT

constexpr int B = 2, D = 512, HQ = 32, HKV = 8, SK = 32768, DH = 128, W = 4096, F = 1792;
constexpr int NW = 8;
constexpr int NT = (NW + 2) * 64;
constexpr unsigned SPIN_CAP = 400000;

constexpr size_t WS_EPOCH = 0;
constexpr size_t WS_GQ = 4096;
constexpr size_t WS_GP = WS_GQ + 4096 * 8;
constexpr size_t WS_GS = WS_GP + 65536 * 8;
constexpr size_t WS_GH = WS_GS + 2048 * 8;
constexpr size_t WS_GA = WS_GH + 8192 * 8;
constexpr size_t WS_STAMP = 4u << 20;

constexpr int VROW = 288;
constexpr int WAREA = 32 * VROW;
constexpr int L_V = 0;
constexpr int L_IMG = L_V + NW * WAREA;
constexpr int L_WQ = L_IMG + 49152;
constexpr int L_XS = L_IMG + 65536;
constexpr int L_HS = L_XS + 4096;
constexpr int L_RED = L_HS + 4096;
constexpr int L_QS = L_RED + 2048;
constexpr int L_ML = L_QS + 5 * 256;
constexpr int L_OA = L_ML + NW * 4 * 2 * 4;
constexpr int L_XA = L_OA + 3 * 1024;
constexpr int L_WT = L_XA + 3 * 1024;
constexpr int L_SYNC = L_WT + NW * 64;
constexpr int L_END = L_SYNC + 64;
constexpr int S_TAG = 0, S_QRDY = 1, S_VISS = 2, S_WRDY = 3, S_QPD = 4, S_WBAR = 5;
static_assert(L_END <= 160 * 1024, "LDS");

__device__ __forceinline__ float bf2f(unsigned x) { return __uint_as_float(x << 16); }
__device__ __forceinline__ float lo(unsigned w) { return __uint_as_float(w << 16); }
__device__ __forceinline__ float hi(unsigned w) { return __uint_as_float(w & 0xffff0000u); }
typedef __bf16 bf16x2_t __attribute__((ext_vector_type(2)));
typedef float f32x2_t __attribute__((ext_vector_type(2)));
__device__ __forceinline__ unsigned pk2(float a, float b) { return __builtin_bit_cast(unsigned, __builtin_convertvector((f32x2_t{a, b}), bf16x2_t)); }
__device__ __forceinline__ float rbf(float f) { return lo(pk2(f, f)); }

template <int CTRL> __device__ __forceinline__ float dpp(float x) {
  return __builtin_bit_cast(float, __builtin_amdgcn_mov_dpp(__builtin_bit_cast(int, x), CTRL, 0xf, 0xf, true));
}
constexpr int XOR1 = 0xB1, XOR2 = 0x4E, ROR4 = 0x124, ROR8 = 0x128;
__device__ __forceinline__ float x16_sum(float x) {
  auto s = __builtin_amdgcn_permlane16_swap(__float_as_uint(x), __float_as_uint(x), false, false);
  return __uint_as_float(s[0]) + __uint_as_float(s[1]);
}
__device__ __forceinline__ float x32_sum(float x) {
  auto t = __builtin_amdgcn_permlane32_swap(__float_as_uint(x), __float_as_uint(x), false, false);
  return __uint_as_float(t[0]) + __uint_as_float(t[1]);
}
__device__ __forceinline__ float x16_max(float x) {
  auto s = __builtin_amdgcn_permlane16_swap(__float_as_uint(x), __float_as_uint(x), false, false);
  return fmaxf(__uint_as_float(s[0]), __uint_as_float(s[1]));
}
__device__ __forceinline__ float x32_max(float x) {
  auto s = __builtin_amdgcn_permlane32_swap(__float_as_uint(x), __float_as_uint(x), false, false);
  return fmaxf(__uint_as_float(s[0]), __uint_as_float(s[1]));
}
__device__ __forceinline__ float row_sum(float v) { v += dpp<XOR1>(v); v += dpp<XOR2>(v); v += dpp<ROR4>(v); v += dpp<ROR8>(v); return v; }
__device__ __forceinline__ float wave_sum(float v) { return x32_sum(x16_sum(row_sum(v))); }

#define LWAIT() asm volatile("s_waitcnt lgkmcnt(0)" ::: "memory")
#define STAMP(i) do { } while (0)
#define STAMP_FLUSH() do { } while (0)
#define STAMP_DECL do { } while (0)

__device__ __forceinline__ void l_arrive(LAS unsigned* p, int lane) {
  LWAIT();
  if (lane == 0) __hip_atomic_fetch_add(p, 1u, __ATOMIC_RELAXED, __HIP_MEMORY_SCOPE_WORKGROUP);
}
__device__ __forceinline__ void l_wait(LAS unsigned* p, unsigned target) {
  for (unsigned spins = 0; __hip_atomic_load(p, __ATOMIC_RELAXED, __HIP_MEMORY_SCOPE_WORKGROUP) < target && spins < (1u << 24); ++spins) __builtin_amdgcn_s_sleep(1);
  asm volatile("" ::: "memory");
}

__device__ __forceinline__ v4u ldnt(const u16* p) { return __builtin_nontemporal_load((const v4u*)p); }
__device__ __forceinline__ void st_gran(gu64* g, unsigned tag, unsigned v) { __hip_atomic_store(g, ((u64)tag << 32) | v, RLX_AGENT); }

template <int N> __device__ __forceinline__ void sweep(gu64* g, int S, unsigned tag, unsigned (&v)[N], int nact) {
  for (unsigned spins = 0;; ++spins) {
    bool ok = true;
    u64 x[N];
#pragma unroll
    for (int k = 0; k < N; ++k) x[k] = __hip_atomic_load(g + (size_t)(k < nact ? k : 0) * S, RLX_AGENT);
#pragma unroll
    for (int k = 0; k < N; ++k) { v[k] = (unsigned)x[k]; ok &= (unsigned)(x[k] >> 32) == tag; }
    if (__all(ok) || spins > SPIN_CAP) return;
  }
}

__device__ __forceinline__ void ln2(const float (&va)[8], const float (&vb)[8], v4u& pa, v4u& pb) {
  float s0 = ((va[0] + va[1]) + (va[2] + va[3])) + ((va[4] + va[5]) + (va[6] + va[7]));
  float s1 = ((vb[0] + vb[1]) + (vb[2] + vb[3])) + ((vb[4] + vb[5]) + (vb[6] + vb[7]));
  s0 = wave_sum(s0); s1 = wave_sum(s1);
  const float m0 = s0 * (1.0f / D), m1 = s1 * (1.0f / D);
  float q0 = 0.f, q1 = 0.f;
#pragma unroll
  for (int i = 0; i < 8; ++i) { const float d0 = va[i] - m0, d1 = vb[i] - m1; q0 = fmaf(d0, d0, q0); q1 = fmaf(d1, d1, q1); }
  q0 = wave_sum(q0); q1 = wave_sum(q1);
  const unsigned mv = pk2(m0, m1), vv = pk2(q0 * (1.0f / D), q1 * (1.0f / D));
  const unsigned ve = pk2(lo(vv) + 1e-5f, hi(vv) + 1e-5f);
  const unsigned sd = pk2(__builtin_sqrtf(lo(ve)), __builtin_sqrtf(hi(ve)));
  const float r0 = 1.0f / lo(sd), r1 = 1.0f / hi(sd), mb0 = lo(mv), mb1 = hi(mv);
#pragma unroll
  for (int i = 0; i < 4; ++i) {
    const unsigned da = pk2(va[2 * i] - mb0, va[2 * i + 1] - mb0), db = pk2(vb[2 * i] - mb1, vb[2 * i + 1] - mb1);
    pa[i] = pk2(lo(da) * r0, hi(da) * r0);
    pb[i] = pk2(lo(db) * r1, hi(db) * r1);
  }
}

template <int OFF0, int OFF1> __device__ __forceinline__ void tr_issue(unsigned addr, v2u& a, v2u& b) {
  asm volatile("ds_read_b64_tr_b16 %0, %2 offset:%c3\n\tds_read_b64_tr_b16 %1, %2 offset:%c4" : "=&v"(a), "=&v"(b) : "v"(addr), "i"(OFF0), "i"(OFF1) : "memory");
}
#define PIN8(t, o) asm volatile("" : "+v"(t[o + 0]), "+v"(t[o + 1]), "+v"(t[o + 2]), "+v"(t[o + 3]), "+v"(t[o + 4]), "+v"(t[o + 5]), "+v"(t[o + 6]), "+v"(t[o + 7]))
#define MFMA(a, b, c) __builtin_amdgcn_mfma_f32_16x16x32_bf16(__builtin_bit_cast(bf16x8, a), __builtin_bit_cast(bf16x8, b), c, 0, 0, 0)
template <int KS, int H> __device__ __forceinline__ void tr_frag8(unsigned addr, v4u (&f)[8]) {
  v2u t[16];
  tr_issue<0 * KS, 0 * KS + H>(addr, t[0], t[1]); tr_issue<1 * KS, 1 * KS + H>(addr, t[2], t[3]);
  tr_issue<2 * KS, 2 * KS + H>(addr, t[4], t[5]); tr_issue<3 * KS, 3 * KS + H>(addr, t[6], t[7]);
  tr_issue<4 * KS, 4 * KS + H>(addr, t[8], t[9]); tr_issue<5 * KS, 5 * KS + H>(addr, t[10], t[11]);
  tr_issue<6 * KS, 6 * KS + H>(addr, t[12], t[13]); tr_issue<7 * KS, 7 * KS + H>(addr, t[14], t[15]);
  LWAIT();
  PIN8(t, 0); PIN8(t, 8);
#pragma unroll
  for (int c = 0; c < 8; ++c) f[c] = v4u{t[2 * c].x, t[2 * c].y, t[2 * c + 1].x, t[2 * c + 1].y};
}
__device__ __forceinline__ void glds16(const u16* gsrc, unsigned lds_dst) {
  unsigned keep;
  asm volatile("s_mov_b32 %0, m0\n\ts_mov_b32 m0, %2\n\ts_nop 0\n\tglobal_load_lds_dwordx4 %1, off nt\n\ts_mov_b32 m0, %0" : "=&s"(keep) : "v"(gsrc), "s"(lds_dst) : "memory");
}

__global__ void __launch_bounds__(NT, 3) mega(const u16* __restrict__ x, const u16* __restrict__ kc, const u16* __restrict__ vc,
                                              const u16* __restrict__ wq, const u16* __restrict__ wo, const u16* __restrict__ wg,
                                              const u16* __restrict__ wu, const u16* __restrict__ wd, u16* __restrict__ out, unsigned char* ws_) {
  extern __shared__ __attribute__((aligned(16))) unsigned char smem[];
  GAS unsigned char* ws = (GAS unsigned char*)ws_;
  const int lane = threadIdx.x & 63, wave0 = __builtin_amdgcn_readfirstlane(threadIdx.x >> 6);
  const int xc = blockIdx.x & 7, r = blockIdx.x >> 3;
  const int kvh = xc, b = r >> 4, s = r & 15;
  const int cbk = kvh * 32 + r;
  const int role = r < 8 ? 0 : r < 22 ? 1 : r < 26 ? 2 : 3;
  const int li = lane & 15, lg = lane >> 4;
  float* XS = (float*)(smem + L_XS); float* HS = (float*)(smem + L_HS);
  float* RED = (float*)(smem + L_RED); float* ML = (float*)(smem + L_ML);
  LAS unsigned* SY = (LAS unsigned*)(smem + L_SYNC);
  const unsigned img_l = (unsigned)(size_t)(LAS unsigned char*)(smem + L_IMG);
  gu64* GQ = (gu64*)(ws + WS_GQ); gu64* GP = (gu64*)(ws + WS_GP); gu64* GS = (gu64*)(ws + WS_GS);
  gu64* GH = (gu64*)(ws + WS_GH); gu64* GA = (gu64*)(ws + WS_GA);
  STAMP_DECL;

  if (wave0 == 0) {
    __builtin_amdgcn_s_setprio(3);
    STAMP(0);
    const v4u xa = *(const v4u*)(x + 8 * lane), xb = *(const v4u*)(x + D + 8 * lane);
    v4u wqr[16];
#pragma unroll
    for (int i = 0; i < 16; ++i) wqr[i] = ldnt(wq + (size_t)((lane >> 1) + 32 * i) * (HQ * DH) + cbk * 16 + (lane & 1) * 8);
    const unsigned ep = __hip_atomic_load((gu32*)(ws + WS_EPOCH), RLX_AGENT);
    __builtin_amdgcn_sched_barrier(0);
    if (lane < 16) SY[lane] = 0u;
    LWAIT();
    __builtin_amdgcn_s_barrier();
    asm volatile("" ::: "memory");
    ((unsigned*)(smem + L_QS + 4 * 256))[lane] = 0u;
    ((v4u*)(smem + L_XA + 2048))[lane] = v4u{0u, 0u, 0u, 0u};
    ((v4u*)(smem + L_OA + 2048))[lane] = v4u{0u, 0u, 0u, 0u};
    {
      float va[8], vb[8];
#pragma unroll
      for (int k = 0; k < 4; ++k) { va[2 * k] = lo(xa[k]); va[2 * k + 1] = hi(xa[k]); vb[2 * k] = lo(xb[k]); vb[2 * k + 1] = hi(xb[k]); }
      *(f32x4*)(XS + 8 * lane) = f32x4{va[0], va[1], va[2], va[3]}; *(f32x4*)(XS + 8 * lane + 4) = f32x4{va[4], va[5], va[6], va[7]};
      *(f32x4*)(XS + D + 8 * lane) = f32x4{vb[0], vb[1], vb[2], vb[3]}; *(f32x4*)(XS + D + 8 * lane + 4) = f32x4{vb[4], vb[5], vb[6], vb[7]};
      v4u pa, pb;
      ln2(va, vb, pa, pb);
      *(v4u*)(smem + L_XA + lane * 16) = pa;
      *(v4u*)(smem + L_XA + 1024 + lane * 16) = pb;
    }
    STAMP(7);
    f32x4 c = {0.f, 0.f, 0.f, 0.f};
    {
      unsigned char* wl = smem + L_WQ;
      const unsigned tr32 = (unsigned)(size_t)(LAS unsigned char*)wl + (8 * lg + (li >> 2)) * 32 + 8 * (li & 3);
      const unsigned char* ab = smem + L_XA + (li < 2 ? li : 2) * 1024 + lg * 16;
#pragma unroll
      for (int hq = 0; hq < 2; ++hq) {
#pragma unroll
        for (int i = 0; i < 8; ++i) *(v4u*)(wl + (32 * (8 * hq + i) + (lane >> 1)) * 32 + (lane & 1) * 16) = wqr[8 * hq + i];
        LWAIT();
        v4u f[8], af[8];
#pragma unroll
        for (int i = 0; i < 8; ++i) af[i] = *(const v4u*)(ab + (8 * hq + i) * 64);
        tr_frag8<1024, 128>(tr32 + hq * 8192, f);
#pragma unroll
        for (int i = 0; i < 8; ++i) c = MFMA(af[i], f[i], c);
      }
    }
    const unsigned tag = __builtin_amdgcn_readfirstlane(ep) + 1u;
    if (lane < 16) st_gran(GQ + cbk * 16 + lane, tag, pk2(c[0], c[1]));
    if (lane == 0) { SY[S_TAG] = tag; SY[S_QPD] = 1u; }
    STAMP(1);
    {
      unsigned v[8];
      sweep<8>(GQ + kvh * 512 + lane, 64, tag, v, 8);
      u16* QS = (u16*)(smem + L_QS);
#pragma unroll
      for (int k = 0; k < 8; ++k) QS[k * 64 + lane] = (u16)(b ? (v[k] >> 16) : (v[k] & 0xffffu));
    }
    l_arrive(SY + S_QRDY, lane);
    STAMP(2);
    STAMP_FLUSH();
    return;
  }

  __builtin_amdgcn_s_barrier();
  asm volatile("" ::: "memory");

  if (wave0 == 1) {
    if (role == 3) return;
    const u16* src; size_t step; int n;
    if (role == 0) { src = wo + (size_t)(xc * 512 + (lane >> 3)) * D + r * 64 + (lane & 7) * 8; step = (size_t)8 * D; n = 64; }
    else if (role == 1) { src = wg + (size_t)(lane >> 1) * F + (xc * 14 + (r - 8)) * 16 + (lane & 1) * 8; step = (size_t)32 * F; n = 16; }
    else { src = wd + (size_t)(lane >> 1) * D + (xc * 4 + (r - 22)) * 16 + (lane & 1) * 8; step = (size_t)32 * D; n = 56; }
    l_wait(SY + S_VISS, NW);
    l_wait(SY + S_QPD, 1u);
    STAMP(8);
    for (int i = 0; i < n; ++i) { glds16(src, img_l + i * 1024); src += step; }
    if (role == 1) {
      src = wu + (size_t)(lane >> 1) * F + (xc * 14 + (r - 8)) * 16 + (lane & 1) * 8;
      for (int i = 0; i < 16; ++i) { glds16(src, img_l + 16384 + i * 1024); src += step; }
    }
    STAMP(9);
    asm volatile("s_waitcnt vmcnt(0)" ::: "memory");
    if (lane == 0) SY[S_WRDY] = 1u;
    STAMP(10);
    STAMP_FLUSH();
    return;
  }

  const int wave = wave0 - 2, tid = threadIdx.x - 128;
  unsigned char* wa = smem + L_V + wave * WAREA;
  const unsigned wa_l = (unsigned)(size_t)(LAS unsigned char*)wa;
  unsigned char* aw = wa + 2048;
  unsigned wbar = 0;
#define WBAR() do { l_arrive(SY + S_WBAR, lane); wbar += NW; l_wait(SY + S_WBAR, wbar); } while (0)
  v4u K[8], V[8];
  const size_t kvbase = ((size_t)(b * HKV + kvh) * SK + (SK - W) + s * 256 + wave * 32) * DH;
#pragma unroll
  for (int t = 0; t < 2; ++t)
#pragma unroll
    for (int kk = 0; kk < 4; ++kk) K[t * 4 + kk] = ldnt(kc + kvbase + (size_t)(t * 16 + li) * DH + kk * 32 + lg * 8);
#pragma unroll
  for (int j = 0; j < 8; ++j) V[j] = ldnt(vc + kvbase + (size_t)(4 * j + lg) * DH + li * 8);
  asm volatile("" ::: "memory");
  l_arrive(SY + S_VISS, lane);
  v4u vf[8];
  {
#pragma unroll
    for (int j = 0; j < 8; ++j) *(v4u*)(wa + (4 * j + lg) * VROW + li * 16) = V[j];
    LWAIT();
    if (wave == 0) STAMP(11);
    if (wave == 7) STAMP(15);
    tr_frag8<32, 16 * VROW>(wa_l + (4 * lg + (li >> 2)) * VROW + 8 * (li & 3), vf);
  }
  if (lane < 32) ((v4u*)(aw + 1024))[lane] = v4u{0u, 0u, 0u, 0u};
  l_wait(SY + S_QRDY, 1u);
  if (wave == 0) STAMP(13);
  const unsigned tag = __builtin_amdgcn_readfirstlane(SY[S_TAG]);
  {
    f32x4 c[2];
    v4u qb[4];
#pragma unroll
    for (int kk = 0; kk < 4; ++kk) qb[kk] = *(const v4u*)(smem + L_QS + (li < 4 ? li : 4) * 256 + kk * 64 + lg * 16);
#pragma unroll
    for (int t = 0; t < 2; ++t) {
      c[t] = f32x4{0.f, 0.f, 0.f, 0.f};
#pragma unroll
      for (int kk = 0; kk < 4; ++kk) c[t] = MFMA(K[t * 4 + kk], qb[kk], c[t]);
    }
    float sc[8];
    constexpr float SCL = 0.08838834764831845f;
#pragma unroll
    for (int t = 0; t < 2; ++t)
#pragma unroll
      for (int p = 0; p < 2; ++p) {
        const unsigned a = pk2(c[t][2 * p], c[t][2 * p + 1]);
        const unsigned bq = pk2(lo(a) * SCL, hi(a) * SCL);
        sc[4 * t + 2 * p] = lo(bq); sc[4 * t + 2 * p + 1] = hi(bq);
      }
    float mx = fmaxf(fmaxf(fmaxf(sc[0], sc[1]), fmaxf(sc[2], sc[3])), fmaxf(fmaxf(sc[4], sc[5]), fmaxf(sc[6], sc[7])));
    mx = x32_max(x16_max(mx));
    v4u pf;
    float l = 0.f;
#pragma unroll
    for (int p = 0; p < 4; ++p) {
      const unsigned pp = pk2(__expf(sc[2 * p] - mx), __expf(sc[2 * p + 1] - mx));
      l += lo(pp) + hi(pp);
      pf[p] = li < 4 ? pp : 0u;
    }
    l = x32_sum(x16_sum(l));
    if (lane < 4) *(float2*)(ML + (wave * 4 + lane) * 2) = make_float2(mx, l);
    float* opw = (float*)wa + (li & 3) * 128 + 4 * lg;
#pragma unroll
    for (int cc = 0; cc < 8; ++cc) {
      const f32x4 o = MFMA(vf[cc], pf, (f32x4{0.f, 0.f, 0.f, 0.f}));
      if (li < 4) *(f32x4*)(opw + cc * 16) = o;
    }
  }
  if (wave == 0) STAMP(14);
  WBAR();
  if (wave == 0) STAMP(3);
  if (tid < 256) {
    const int h = tid >> 6, dp = tid & 63;
    float mw[NW], lw[NW];
#pragma unroll
    for (int w = 0; w < NW; ++w) { const float2 a = *(const float2*)(ML + (w * 4 + h) * 2); mw[w] = a.x; lw[w] = a.y; }
    const float M = fmaxf(fmaxf(fmaxf(mw[0], mw[1]), fmaxf(mw[2], mw[3])), fmaxf(fmaxf(mw[4], mw[5]), fmaxf(mw[6], mw[7])));
    float L = 0.f, o0 = 0.f, o1 = 0.f;
#pragma unroll
    for (int w = 0; w < NW; ++w) {
      const float wt = __expf(mw[w] - M);
      L += wt * lw[w];
      const float2 a = *(const float2*)((const float*)(smem + L_V + w * WAREA) + h * 128 + 2 * dp);
      o0 += wt * a.x; o1 += wt * a.y;
    }
    const float inv = __builtin_amdgcn_rcpf(L);
    const int slot = (b * 8 + kvh) * 16 + s;
    st_gran(GP + slot * 256 + tid, tag, pk2(o0 * inv, o1 * inv));
    if (dp == 0) { st_gran(GS + slot * 8 + h * 2, tag, __float_as_uint(M)); st_gran(GS + slot * 8 + h * 2 + 1, tag, __float_as_uint(L)); }
  }
  if (wave == 0) STAMP(4);
  if (role == 3) { STAMP_FLUSH(); return; }

  v4u bf[8];
  l_wait(SY + S_WRDY, 1u);
  if (wave == 0) STAMP(12);

  if (role == 0) {
    tr_frag8<4096, 512>(img_l + ((wave >> 2) * 256 + 8 * lg + (li >> 2)) * 128 + (wave & 3) * 32 + 8 * (li & 3), bf);
    const int bb = wave >> 2, h = wave & 3;
    unsigned v[17];
    {
      gu64* g0 = GP + (size_t)((bb * 8 + xc) * 16) * 256 + h * 64 + lane;
      gu64* g1 = GS + (size_t)((bb * 8 + xc) * 16 + ((lane & 31) >> 1)) * 8 + h * 2 + (lane & 1);
      for (unsigned spins = 0;; ++spins) {
        bool ok = true;
        u64 xx[17];
#pragma unroll
        for (int k = 0; k < 16; ++k) xx[k] = __hip_atomic_load(g0 + k * 256, RLX_AGENT);
        xx[16] = __hip_atomic_load(g1, RLX_AGENT);
#pragma unroll
        for (int k = 0; k < 17; ++k) { v[k] = (unsigned)xx[k]; ok &= (unsigned)(xx[k] >> 32) == tag; }
        if (__all(ok) || spins > SPIN_CAP) break;
      }
    }
    if (wave == 0) STAMP(5);
    {
      const float val = __uint_as_float(v[16]);
      const float swp = dpp<XOR1>(val);
      const float m = (lane & 1) ? swp : val, l = (lane & 1) ? val : swp;
      float M = m; M = fmaxf(M, dpp<XOR2>(M)); M = fmaxf(M, dpp<ROR4>(M)); M = fmaxf(M, dpp<ROR8>(M)); M = x16_max(M);
      const float wgt = l * __expf(m - M);
      float L = wgt; L += dpp<XOR2>(L); L += dpp<ROR4>(L); L += dpp<ROR8>(L); L = x16_sum(L);
      float* WT = (float*)(smem + L_WT + wave * 64);
      if (lane < 32 && !(lane & 1)) WT[lane >> 1] = wgt * __builtin_amdgcn_rcpf(L);
      LWAIT();
      float o0 = 0.f, o1 = 0.f;
#pragma unroll
      for (int q4 = 0; q4 < 4; ++q4) {
        const f32x4 w4 = *(const f32x4*)(WT + 4 * q4);
#pragma unroll
        for (int j = 0; j < 4; ++j) { o0 += w4[j] * lo(v[4 * q4 + j]); o1 += w4[j] * hi(v[4 * q4 + j]); }
      }
      *(unsigned*)(smem + L_OA + bb * 1024 + h * 256 + lane * 4) = pk2(o0, o1);
    }
    if (wave == 0) STAMP(16);
    WBAR();
    if (wave == 0) STAMP(17);
    {
      const int kh = wave >> 2;
      const unsigned char* ab = smem + L_OA + (li < 2 ? li : 2) * 1024 + kh * 512 + lg * 16;
      f32x4 c = {0.f, 0.f, 0.f, 0.f};
#pragma unroll
      for (int ks = 0; ks < 8; ++ks) c = MFMA(*(const v4u*)(ab + ks * 64), bf[ks], c);
      if (lg == 0) { RED[wave * 64 + li] = c[0]; RED[wave * 64 + 16 + li] = c[1]; }
    }
    if (wave == 0) STAMP(18);
    WBAR();
    if (wave == 0) STAMP(19);
    if (tid < 128) {
      const int q = tid >> 6, col = tid & 63, nt = col >> 4, ci = col & 15;
      const float a = RED[nt * 64 + q * 16 + ci] + RED[(4 + nt) * 64 + q * 16 + ci];
      st_gran(GH + (xc * 2 + q) * 512 + r * 64 + col, tag, __float_as_uint(a));
    }
    if (wave == 0) STAMP(6);
    STAMP_FLUSH();
    return;
  }

  if (role == 1) {
    tr_frag8<1024, 128>(img_l + (wave & 1) * 16384 + ((wave >> 1) * 128 + 8 * lg + (li >> 2)) * 32 + 8 * (li & 3), bf);
    const int fb = xc * 14 + (r - 8);
    unsigned v[16];
    sweep<16>(GH + tid, 512, tag, v, 16);
    if (wave == 0) STAMP(5);
    float s0 = 0.f, s1 = 0.f;
#pragma unroll
    for (int k = 0; k < 8; ++k) { s0 += __uint_as_float(v[2 * k]); s1 += __uint_as_float(v[2 * k + 1]); }
    {
      const unsigned sp = pk2(s0, s1);
      const unsigned hp = pk2(XS[tid] + lo(sp), XS[D + tid] + hi(sp));
      HS[tid] = lo(hp); HS[D + tid] = hi(hp);
    }
    if (wave == 0) STAMP(16);
    WBAR();
    if (wave == 0) STAMP(17);
    {
      float va[8], vb[8];
      const f32x4 a0 = *(const f32x4*)(HS + 8 * lane), a1 = *(const f32x4*)(HS + 8 * lane + 4);
      const f32x4 b0 = *(const f32x4*)(HS + D + 8 * lane), b1 = *(const f32x4*)(HS + D + 8 * lane + 4);
#pragma unroll
      for (int k = 0; k < 4; ++k) { va[k] = a0[k]; va[4 + k] = a1[k]; vb[k] = b0[k]; vb[4 + k] = b1[k]; }
      v4u pa, pb;
      ln2(va, vb, pa, pb);
      if (lg == (wave >> 1)) {
        *(v4u*)(aw + li * 16) = pa;
        *(v4u*)(aw + 512 + li * 16) = pb;
      }
      LWAIT();
      const unsigned char* ab = aw + (li < 2 ? li : 2) * 512 + lg * 16;
      f32x4 c = {0.f, 0.f, 0.f, 0.f};
#pragma unroll
      for (int ks = 0; ks < 4; ++ks) c = MFMA(*(const v4u*)(ab + ks * 64), bf[ks], c);
      if (lg == 0) { RED[wave * 64 + li] = c[0]; RED[wave * 64 + 16 + li] = c[1]; }
    }
    if (wave == 0) STAMP(18);
    WBAR();
    if (wave == 0) STAMP(19);
    if (tid < 16) {
      float g0 = 0.f, u0 = 0.f, g1 = 0.f, u1 = 0.f;
#pragma unroll
      for (int kq = 0; kq < 4; ++kq) {
        g0 += RED[(2 * kq) * 64 + tid]; u0 += RED[(2 * kq + 1) * 64 + tid];
        g1 += RED[(2 * kq) * 64 + 16 + tid]; u1 += RED[(2 * kq + 1) * 64 + 16 + tid];
      }
      const unsigned gp = pk2(g0, g1), up = pk2(u0, u1);
      const unsigned sp = pk2(1.0f / (1.0f + __expf(-lo(gp))), 1.0f / (1.0f + __expf(-hi(gp))));
      st_gran(GA + fb * 16 + tid, tag, pk2(lo(sp) * lo(up), hi(sp) * hi(up)));
    }
    if (wave == 0) STAMP(6);
    STAMP_FLUSH();
    return;
  }

  {
    tr_frag8<1024, 128>(img_l + (wave * 224 + 8 * lg + (li >> 2)) * 32 + 8 * (li & 3), bf);
    const int cb = xc * 4 + (r - 22);
    float hres = 0.f;
    if (wave == 0) {
      unsigned v[8];
      const int q = (lane >> 4) & 1, c = lane & 15;
      sweep<8>(GH + q * 512 + cb * 16 + c, 1024, tag, v, 8);
      float a = 0.f;
#pragma unroll
      for (int k = 0; k < 8; ++k) a += __uint_as_float(v[k]);
      hres = rbf(XS[q * D + cb * 16 + c] + rbf(a));
    }
    {
      unsigned v[4];
      sweep<4>(GA + wave * 224 + lane, 64, tag, v, lane < 32 ? 4 : 3);
      u16* A0 = (u16*)aw; u16* A1 = (u16*)(aw + 512);
#pragma unroll
      for (int j = 0; j < 4; ++j) if (j < 3 || lane < 32) { A0[j * 64 + lane] = (u16)(v[j] & 0xffffu); A1[j * 64 + lane] = (u16)(v[j] >> 16); }
    }
    if (wave == 0) STAMP(5);
    LWAIT();
    {
      const unsigned char* ab = aw + (li < 2 ? li : 2) * 512 + lg * 16;
      f32x4 c = {0.f, 0.f, 0.f, 0.f};
#pragma unroll
      for (int ks = 0; ks < 7; ++ks) c = MFMA(*(const v4u*)(ab + ks * 64), bf[ks], c);
      if (lg == 0) { RED[wave * 64 + li] = c[0]; RED[wave * 64 + 16 + li] = c[1]; }
    }
    WBAR();
    if (tid < 32) {
      float a = 0.f;
#pragma unroll
      for (int w = 0; w < NW; ++w) a += RED[w * 64 + tid];
      const int q = tid >> 4, c = tid & 15;
      out[q * D + cb * 16 + c] = (u16)(pk2(hres + rbf(a), 0.f) & 0xffffu);
      if (cb == 0 && tid == 0) __hip_atomic_store((gu32*)(ws + WS_EPOCH), tag, RLX_AGENT);
    }
    if (wave == 0) STAMP(6);
    STAMP_FLUSH();
  }
}

extern "C" void kernel_launch(void* const* d_in, const int* in_sizes, int n_in, void* d_out, int out_size, void* d_ws, size_t ws_size,
                              hipStream_t stream) {
  (void)hipFuncSetAttribute((const void*)mega, hipFuncAttributeMaxDynamicSharedMemorySize, L_END);
  mega<<<256, NT, L_END, stream>>>((const u16*)d_in[0], (const u16*)d_in[1], (const u16*)d_in[2], (const u16*)d_in[3], (const u16*)d_in[4],
                                   (const u16*)d_in[6], (const u16*)d_in[7], (const u16*)d_in[8], (u16*)d_out, (unsigned char*)d_ws);
}
```

```cpp
#include <hip/hip_runtime.h>
#include <stdint.h>

typedef unsigned short u16;
typedef unsigned long long u64;
typedef unsigned v4u __attribute__((ext_vector_type(4)));
typedef unsigned v2u __attribute__((ext_vector_type(2)));
typedef float f32x4 __attribute__((ext_vector_type(4)));
typedef short bf16x8 __attribute__((ext_vector_type(8)));
#define GAS __attribute__((address_space(1)))
#define LAS __attribute__((address_space(3)))
typedef GAS u64 gu64;
typedef GAS unsigned gu32;
#define RLX_AGENT __ATOMIC_RELAXED, __HIP_MEMORY_SCOPE_AGENT

constexpr int B = 2, D = 512, HQ = 32, HKV = 8, SK = 32768, DH = 128, W = 4096, F = 1792;
constexpr int NW = 8;
constexpr int NT = (NW + 1) * 64;
constexpr unsigned SPIN_CAP = 400000;
#define WQSIG 1
#define KWIN 16

constexpr size_t WS_EPOCH = 0;
constexpr size_t WS_GQ = 4096;
constexpr size_t WS_GP = WS_GQ + 4096 * 8;
constexpr size_t WS_GS = WS_GP + 65536 * 8;
constexpr size_t WS_GH = WS_GS + 2048 * 8;
constexpr size_t WS_GA = WS_GH + 8192 * 8;
constexpr size_t WS_STAMP = 4u << 20;

constexpr int VROW = 288;
constexpr int WAREA = 32 * VROW;
constexpr int L_V = 0;
constexpr int L_IMG = L_V + NW * WAREA;
constexpr int L_WQ = L_IMG + 49152;
constexpr int L_XS = L_IMG + 65536;
constexpr int L_RED = L_XS + 4096;
constexpr int L_QS = L_RED + 2048;
constexpr int L_ML = L_QS + 5 * 256;
constexpr int L_OA = L_ML + NW * 4 * 2 * 4;
constexpr int L_XA = L_OA + 3 * 1024;
constexpr int L_WT = L_XA + 3 * 1024;
constexpr int L_PF = L_WT + NW * 64;
constexpr int L_HS = L_PF;
constexpr int L_SYNC = L_PF + NW * 1024;
constexpr int L_END = L_SYNC + 64;
constexpr int S_TAG = 0, S_QRDY = 1, S_VISS = 2, S_WRDY = 3, S_QPD = 4, S_WBAR = 5;
static_assert(L_END <= 160 * 1024, "LDS");

__device__ __forceinline__ float bf2f(unsigned x) { return __uint_as_float(x << 16); }
__device__ __forceinline__ float lo(unsigned w) { return __uint_as_float(w << 16); }
__device__ __forceinline__ float hi(unsigned w) { return __uint_as_float(w & 0xffff0000u); }
typedef __bf16 bf16x2_t __attribute__((ext_vector_type(2)));
typedef float f32x2_t __attribute__((ext_vector_type(2)));
__device__ __forceinline__ unsigned pk2(float a, float b) { return __builtin_bit_cast(unsigned, __builtin_convertvector((f32x2_t{a, b}), bf16x2_t)); }
__device__ __forceinline__ float rbf(float f) { return lo(pk2(f, f)); }

template <int CTRL> __device__ __forceinline__ float dpp(float x) {
  return __builtin_bit_cast(float, __builtin_amdgcn_mov_dpp(__builtin_bit_cast(int, x), CTRL, 0xf, 0xf, true));
}
constexpr int XOR1 = 0xB1, XOR2 = 0x4E, ROR4 = 0x124, ROR8 = 0x128;
__device__ __forceinline__ float x16_sum(float x) {
  auto s = __builtin_amdgcn_permlane16_swap(__float_as_uint(x), __float_as_uint(x), false, false);
  return __uint_as_float(s[0]) + __uint_as_float(s[1]);
}
__device__ __forceinline__ float x32_sum(float x) {
  auto t = __builtin_amdgcn_permlane32_swap(__float_as_uint(x), __float_as_uint(x), false, false);
  return __uint_as_float(t[0]) + __uint_as_float(t[1]);
}
__device__ __forceinline__ float x16_max(float x) {
  auto s = __builtin_amdgcn_permlane16_swap(__float_as_uint(x), __float_as_uint(x), false, false);
  return fmaxf(__uint_as_float(s[0]), __uint_as_float(s[1]));
}
__device__ __forceinline__ float x32_max(float x) {
  auto s = __builtin_amdgcn_permlane32_swap(__float_as_uint(x), __float_as_uint(x), false, false);
  return fmaxf(__uint_as_float(s[0]), __uint_as_float(s[1]));
}
__device__ __forceinline__ float row_sum(float v) { v += dpp<XOR1>(v); v += dpp<XOR2>(v); v += dpp<ROR4>(v); v += dpp<ROR8>(v); return v; }
__device__ __forceinline__ float wave_sum(float v) { return x32_sum(x16_sum(row_sum(v))); }

#define LWAIT() asm volatile("s_waitcnt lgkmcnt(0)" ::: "memory")
#define STAMP(i) do { } while (0)
#define STAMP_FLUSH() do { } while (0)
#define STAMP_DECL do { } while (0)

__device__ __forceinline__ void l_arrive(LAS unsigned* p, int lane) {
  LWAIT();
  if (lane == 0) __hip_atomic_fetch_add(p, 1u, __ATOMIC_RELAXED, __HIP_MEMORY_SCOPE_WORKGROUP);
}
__device__ __forceinline__ void l_wait(LAS unsigned* p, unsigned target) {
  for (unsigned spins = 0; __hip_atomic_load(p, __ATOMIC_RELAXED, __HIP_MEMORY_SCOPE_WORKGROUP) < target && spins < (1u << 24); ++spins) __builtin_amdgcn_s_sleep(1);
  asm volatile("" ::: "memory");
}

__device__ __forceinline__ v4u ldnt(const u16* p) { return __builtin_nontemporal_load((const v4u*)p); }
__device__ __forceinline__ void st_gran(gu64* g, unsigned tag, unsigned v) { __hip_atomic_store(g, ((u64)tag << 32) | v, RLX_AGENT); }

template <int N> __device__ __forceinline__ void sweep(gu64* g, int S, unsigned tag, unsigned (&v)[N], int nact) {
  for (unsigned spins = 0;; ++spins) {
    bool ok = true;
    u64 x[N];
#pragma unroll
    for (int k = 0; k < N; ++k) x[k] = __hip_atomic_load(g + (size_t)(k < nact ? k : 0) * S, RLX_AGENT);
#pragma unroll
    for (int k = 0; k < N; ++k) { v[k] = (unsigned)x[k]; ok &= (unsigned)(x[k] >> 32) == tag; }
    if (__all(ok) || spins > SPIN_CAP) return;
    __builtin_amdgcn_s_sleep(2);
  }
}

__device__ __forceinline__ void poll1(gu64* g, unsigned tag) {
  for (unsigned spins = 0;; ++spins) {
    const u64 x = __hip_atomic_load(g, RLX_AGENT);
    if (__all((unsigned)(x >> 32) == tag) || spins > SPIN_CAP) return;
    __builtin_amdgcn_s_sleep(2);
  }
}
__device__ __forceinline__ void ln2(const float (&va)[8], const float (&vb)[8], v4u& pa, v4u& pb) {
  float s0 = ((va[0] + va[1]) + (va[2] + va[3])) + ((va[4] + va[5]) + (va[6] + va[7]));
  float s1 = ((vb[0] + vb[1]) + (vb[2] + vb[3])) + ((vb[4] + vb[5]) + (vb[6] + vb[7]));
  s0 = wave_sum(s0); s1 = wave_sum(s1);
  const float m0 = s0 * (1.0f / D), m1 = s1 * (1.0f / D);
  float q0 = 0.f, q1 = 0.f;
#pragma unroll
  for (int i = 0; i < 8; ++i) { const float d0 = va[i] - m0, d1 = vb[i] - m1; q0 = fmaf(d0, d0, q0); q1 = fmaf(d1, d1, q1); }
  q0 = wave_sum(q0); q1 = wave_sum(q1);
  const unsigned mv = pk2(m0, m1), vv = pk2(q0 * (1.0f / D), q1 * (1.0f / D));
  const unsigned ve = pk2(lo(vv) + 1e-5f, hi(vv) + 1e-5f);
  const unsigned sd = pk2(__builtin_sqrtf(lo(ve)), __builtin_sqrtf(hi(ve)));
  const float r0 = 1.0f / lo(sd), r1 = 1.0f / hi(sd), mb0 = lo(mv), mb1 = hi(mv);
#pragma unroll
  for (int i = 0; i < 4; ++i) {
    const unsigned da = pk2(va[2 * i] - mb0, va[2 * i + 1] - mb0), db = pk2(vb[2 * i] - mb1, vb[2 * i + 1] - mb1);
    pa[i] = pk2(lo(da) * r0, hi(da) * r0);
    pb[i] = pk2(lo(db) * r1, hi(db) * r1);
  }
}

template <int OFF0, int OFF1> __device__ __forceinline__ void tr_issue(unsigned addr, v2u& a, v2u& b) {
  asm volatile("ds_read_b64_tr_b16 %0, %2 offset:%c3\n\tds_read_b64_tr_b16 %1, %2 offset:%c4" : "=&v"(a), "=&v"(b) : "v"(addr), "i"(OFF0), "i"(OFF1) : "memory");
}
#define PIN8(t, o) asm volatile("" : "+v"(t[o + 0]), "+v"(t[o + 1]), "+v"(t[o + 2]), "+v"(t[o + 3]), "+v"(t[o + 4]), "+v"(t[o + 5]), "+v"(t[o + 6]), "+v"(t[o + 7]))
#define MFMA(a, b, c) __builtin_amdgcn_mfma_f32_16x16x32_bf16(__builtin_bit_cast(bf16x8, a), __builtin_bit_cast(bf16x8, b), c, 0, 0, 0)
template <int KS, int H> __device__ __forceinline__ void tr_frag8(unsigned addr, v4u (&f)[8]) {
  v2u t[16];
  tr_issue<0 * KS, 0 * KS + H>(addr, t[0], t[1]); tr_issue<1 * KS, 1 * KS + H>(addr, t[2], t[3]);
  tr_issue<2 * KS, 2 * KS + H>(addr, t[4], t[5]); tr_issue<3 * KS, 3 * KS + H>(addr, t[6], t[7]);
  tr_issue<4 * KS, 4 * KS + H>(addr, t[8], t[9]); tr_issue<5 * KS, 5 * KS + H>(addr, t[10], t[11]);
  tr_issue<6 * KS, 6 * KS + H>(addr, t[12], t[13]); tr_issue<7 * KS, 7 * KS + H>(addr, t[14], t[15]);
  LWAIT();
  PIN8(t, 0); PIN8(t, 8);
#pragma unroll
  for (int c = 0; c < 8; ++c) f[c] = v4u{t[2 * c].x, t[2 * c].y, t[2 * c + 1].x, t[2 * c + 1].y};
}
__device__ __forceinline__ void glds16(const u16* gsrc, unsigned lds_dst) {
  unsigned keep;
  asm volatile("s_mov_b32 %0, m0\n\ts_mov_b32 m0, %2\n\ts_nop 0\n\tglobal_load_lds_dwordx4 %1, off nt\n\ts_mov_b32 m0, %0" : "=&s"(keep) : "v"(gsrc), "s"(lds_dst) : "memory");
}

__global__ void __launch_bounds__(NT, 3) mega(const u16* __restrict__ x, const u16* __restrict__ kc, const u16* __restrict__ vc,
                                              const u16* __restrict__ wq, const u16* __restrict__ wo, const u16* __restrict__ wg,
                                              const u16* __restrict__ wu, const u16* __restrict__ wd, u16* __restrict__ out, unsigned char* ws_) {
  extern __shared__ __attribute__((aligned(16))) unsigned char smem[];
  GAS unsigned char* ws = (GAS unsigned char*)ws_;
  const int lane = threadIdx.x & 63, wave0 = __builtin_amdgcn_readfirstlane(threadIdx.x >> 6);
  const int xc = blockIdx.x & 7, r = blockIdx.x >> 3;
  const int kvh = xc, b = r >> 4, s = r & 15;
  const int cbk = kvh * 32 + r;
  const int role = r < 8 ? 0 : r < 22 ? 1 : r < 26 ? 2 : 3;
  const int li = lane & 15, lg = lane >> 4;
  float* XS = (float*)(smem + L_XS); float* HS = (float*)(smem + L_HS);
  float* RED = (float*)(smem + L_RED); float* ML = (float*)(smem + L_ML);
  LAS unsigned* SY = (LAS unsigned*)(smem + L_SYNC);
  const unsigned img_l = (unsigned)(size_t)(LAS unsigned char*)(smem + L_IMG);
  gu64* GQ = (gu64*)(ws + WS_GQ); gu64* GP = (gu64*)(ws + WS_GP); gu64* GS = (gu64*)(ws + WS_GS);
  gu64* GH = (gu64*)(ws + WS_GH); gu64* GA = (gu64*)(ws + WS_GA);
  STAMP_DECL;

  if (wave0 == 0) {
    __builtin_amdgcn_s_setprio(3);
    STAMP(0);
    const v4u xa = *(const v4u*)(x + 8 * lane), xb = *(const v4u*)(x + D + 8 * lane);
    v4u wqr[16];
#pragma unroll
    for (int i = 0; i < 16; ++i) wqr[i] = ldnt(wq + (size_t)((lane >> 1) + 32 * i) * (HQ * DH) + cbk * 16 + (lane & 1) * 8);
    const unsigned ep = __hip_atomic_load((gu32*)(ws + WS_EPOCH), RLX_AGENT);
    __builtin_amdgcn_sched_barrier(0);
    if (lane < 16) SY[lane] = 0u;
    LWAIT();
    __builtin_amdgcn_s_barrier();
    asm volatile("" ::: "memory");
    ((unsigned*)(smem + L_QS + 4 * 256))[lane] = 0u;
    ((v4u*)(smem + L_XA + 2048))[lane] = v4u{0u, 0u, 0u, 0u};
    ((v4u*)(smem + L_OA + 2048))[lane] = v4u{0u, 0u, 0u, 0u};
    {
      float va[8], vb[8];
#pragma unroll
      for (int k = 0; k < 4; ++k) { va[2 * k] = lo(xa[k]); va[2 * k + 1] = hi(xa[k]); vb[2 * k] = lo(xb[k]); vb[2 * k + 1] = hi(xb[k]); }
      *(f32x4*)(XS + 8 * lane) = f32x4{va[0], va[1], va[2], va[3]}; *(f32x4*)(XS + 8 * lane + 4) = f32x4{va[4], va[5], va[6], va[7]};
      *(f32x4*)(XS + D + 8 * lane) = f32x4{vb[0], vb[1], vb[2], vb[3]}; *(f32x4*)(XS + D + 8 * lane + 4) = f32x4{vb[4], vb[5], vb[6], vb[7]};
      v4u pa, pb;
      ln2(va, vb, pa, pb);
      *(v4u*)(smem + L_XA + lane * 16) = pa;
      *(v4u*)(smem + L_XA + 1024 + lane * 16) = pb;
    }
    STAMP(7);
    f32x4 c = {0.f, 0.f, 0.f, 0.f};
    {
      unsigned char* wl = smem + L_WQ;
      const unsigned tr32 = (unsigned)(size_t)(LAS unsigned char*)wl + (8 * lg + (li >> 2)) * 32 + 8 * (li & 3);
      const unsigned char* ab = smem + L_XA + (li < 2 ? li : 2) * 1024 + lg * 16;
#pragma unroll
      for (int hq = 0; hq < 2; ++hq) {
#pragma unroll
        for (int i = 0; i < 8; ++i) *(v4u*)(wl + (32 * (8 * hq + i) + (lane >> 1)) * 32 + (lane & 1) * 16) = wqr[8 * hq + i];
        LWAIT();
        if (hq == WQSIG && lane == 0) SY[S_VISS] = 1u;
        v4u f[8], af[8];
#pragma unroll
        for (int i = 0; i < 8; ++i) af[i] = *(const v4u*)(ab + (8 * hq + i) * 64);
        tr_frag8<1024, 128>(tr32 + hq * 8192, f);
#pragma unroll
        for (int i = 0; i < 8; ++i) c = MFMA(af[i], f[i], c);
      }
    }
    const unsigned tag = __builtin_amdgcn_readfirstlane(ep) + 1u;
    if (lane < 16) st_gran(GQ + cbk * 16 + lane, tag, pk2(c[0], c[1]));
    if (lane == 0) { SY[S_TAG] = tag; SY[S_QPD] = 1u; }
    STAMP(1);
    {
      unsigned v[8];
      sweep<8>(GQ + kvh * 512 + lane, 64, tag, v, 8);
      u16* QS = (u16*)(smem + L_QS);
#pragma unroll
      for (int k = 0; k < 8; ++k) QS[k * 64 + lane] = (u16)(b ? (v[k] >> 16) : (v[k] & 0xffffu));
    }
    l_arrive(SY + S_QRDY, lane);
    STAMP(2);
    STAMP_FLUSH();
    return;
  }

  __builtin_amdgcn_s_barrier();
  asm volatile("" ::: "memory");

  const int wave = wave0 - 1, tid = threadIdx.x - 64;
  unsigned char* wa = smem + L_V + wave * WAREA;
  const unsigned wa_l = (unsigned)(size_t)(LAS unsigned char*)wa;
  unsigned char* aw = wa + 4096;
#define WBAR() do { LWAIT(); __builtin_amdgcn_s_barrier(); asm volatile("" ::: "memory"); } while (0)
  v4u K[8], V[8];
  const size_t kvbase = ((size_t)(b * HKV + kvh) * SK + (SK - W) + s * 256 + wave * 32) * DH + (size_t)lg * DH + li * 8;
  l_wait(SY + S_VISS, 1u);
#pragma unroll
  for (int j = 0; j < 16; ++j) {
    if (j >= KWIN) { if (j - KWIN < 8) asm volatile("" :: "v"(K[j - KWIN]) : "memory"); else asm volatile("" :: "v"(V[j - KWIN - 8]) : "memory"); }
    if (j < 8) K[j] = ldnt(kc + kvbase + (size_t)4 * j * DH); else V[j - 8] = ldnt(vc + kvbase + (size_t)4 * (j - 8) * DH);
  }
  asm volatile("" ::: "memory");
  v4u kf[8];
  {
#pragma unroll
    for (int j = 0; j < 8; ++j) *(v4u*)(wa + (4 * j + lg) * VROW + li * 16) = K[j];
    LWAIT();
#pragma unroll
    for (int t = 0; t < 2; ++t)
#pragma unroll
      for (int kk = 0; kk < 4; ++kk) kf[t * 4 + kk] = *(const v4u*)(wa + (t * 16 + li) * VROW + kk * 64 + lg * 16);
    LWAIT();
    asm volatile("" : "+v"(kf[0]), "+v"(kf[1]), "+v"(kf[2]), "+v"(kf[3]), "+v"(kf[4]), "+v"(kf[5]), "+v"(kf[6]), "+v"(kf[7]));
  }
  l_wait(SY + S_QRDY, 1u);
  if (wave == 0) STAMP(13);
  const unsigned tag = __builtin_amdgcn_readfirstlane(SY[S_TAG]);
  {
    f32x4 c[2];
    v4u qb[4];
#pragma unroll
    for (int kk = 0; kk < 4; ++kk) qb[kk] = *(const v4u*)(smem + L_QS + (li < 4 ? li : 4) * 256 + kk * 64 + lg * 16);
#pragma unroll
    for (int t = 0; t < 2; ++t) {
      c[t] = f32x4{0.f, 0.f, 0.f, 0.f};
#pragma unroll
      for (int kk = 0; kk < 4; ++kk) c[t] = MFMA(kf[t * 4 + kk], qb[kk], c[t]);
    }
    float sc[8];
    constexpr float SCL = 0.08838834764831845f;
#pragma unroll
    for (int t = 0; t < 2; ++t)
#pragma unroll
      for (int p = 0; p < 2; ++p) {
        const unsigned a = pk2(c[t][2 * p], c[t][2 * p + 1]);
        const unsigned bq = pk2(lo(a) * SCL, hi(a) * SCL);
        sc[4 * t + 2 * p] = lo(bq); sc[4 * t + 2 * p + 1] = hi(bq);
      }
    float mx = fmaxf(fmaxf(fmaxf(sc[0], sc[1]), fmaxf(sc[2], sc[3])), fmaxf(fmaxf(sc[4], sc[5]), fmaxf(sc[6], sc[7])));
    mx = x32_max(x16_max(mx));
    const float M = mx;
    v4u pf;
    float l = 0.f;
#pragma unroll
    for (int p = 0; p < 4; ++p) {
      const unsigned pp = pk2(__expf(sc[2 * p] - M), __expf(sc[2 * p + 1] - M));
      l += lo(pp) + hi(pp);
      pf[p] = li < 4 ? pp : 0u;
    }
    l = x32_sum(x16_sum(l));
    if (lane < 4) *(float2*)(ML + (wave * 4 + lane) * 2) = make_float2(mx, l);
    *(v4u*)(smem + L_PF + wave * 1024 + lane * 16) = pf;
#pragma unroll
    for (int j = 0; j < 8; ++j) *(v4u*)(wa + (4 * j + lg) * VROW + li * 16) = V[j];
    if (wave == 0) STAMP(11);
    if (wave == 7) STAMP(15);
    WBAR();
    if (wave == 0) STAMP(3);
    const unsigned vb0 = (unsigned)(size_t)(LAS unsigned char*)(smem + L_V) + (4 * lg + (li >> 2)) * VROW + 8 * (li & 3) + 32 * wave;
    v2u t[16];
    tr_issue<0 * WAREA, 0 * WAREA + 16 * VROW>(vb0, t[0], t[1]); tr_issue<1 * WAREA, 1 * WAREA + 16 * VROW>(vb0, t[2], t[3]);
    tr_issue<2 * WAREA, 2 * WAREA + 16 * VROW>(vb0, t[4], t[5]); tr_issue<3 * WAREA, 3 * WAREA + 16 * VROW>(vb0, t[6], t[7]);
    tr_issue<0 * WAREA, 0 * WAREA + 16 * VROW>(vb0 + 4 * WAREA, t[8], t[9]); tr_issue<1 * WAREA, 1 * WAREA + 16 * VROW>(vb0 + 4 * WAREA, t[10], t[11]);
    tr_issue<2 * WAREA, 2 * WAREA + 16 * VROW>(vb0 + 4 * WAREA, t[12], t[13]); tr_issue<3 * WAREA, 3 * WAREA + 16 * VROW>(vb0 + 4 * WAREA, t[14], t[15]);
    v4u pfr[8];
#pragma unroll
    for (int kb = 0; kb < 8; ++kb) pfr[kb] = *(const v4u*)(smem + L_PF + kb * 1024 + lane * 16);
    float mw[NW], lw[NW];
#pragma unroll
    for (int w = 0; w < NW; ++w) { const float2 a = *(const float2*)(ML + (w * 4 + (li & 3)) * 2); mw[w] = a.x; lw[w] = a.y; }
    const float MG = fmaxf(fmaxf(fmaxf(mw[0], mw[1]), fmaxf(mw[2], mw[3])), fmaxf(fmaxf(mw[4], mw[5]), fmaxf(mw[6], mw[7])));
    float L = 0.f;
#pragma unroll
    for (int w = 0; w < NW; ++w) { mw[w] = __expf(mw[w] - MG); L = fmaf(mw[w], lw[w], L); }
    LWAIT();
    PIN8(t, 0); PIN8(t, 8);
    f32x4 o = {0.f, 0.f, 0.f, 0.f};
#pragma unroll
    for (int kb = 0; kb < 8; ++kb) {
      const f32x4 ob = MFMA((v4u{t[2 * kb].x, t[2 * kb].y, t[2 * kb + 1].x, t[2 * kb + 1].y}), pfr[kb], (f32x4{0.f, 0.f, 0.f, 0.f}));
#pragma unroll
      for (int e = 0; e < 4; ++e) o[e] = fmaf(mw[kb], ob[e], o[e]);
    }
    const float inv = __builtin_amdgcn_rcpf(L);
    const int slot = (b * 8 + kvh) * 16 + s;
    if (li < 4) {
      gu64* g = GP + slot * 256 + li * 64 + 8 * wave + 2 * lg;
      st_gran(g, tag, pk2(o[0] * inv, o[1] * inv));
      st_gran(g + 1, tag, pk2(o[2] * inv, o[3] * inv));
      if (wave == 0 && lg == 0) { st_gran(GS + slot * 8 + li * 2, tag, __float_as_uint(MG)); st_gran(GS + slot * 8 + li * 2 + 1, tag, __float_as_uint(L)); }
    }
  }
  if (wave == 0) STAMP(4);
  if (role == 3) { STAMP_FLUSH(); return; }

  {
    const u16* src; size_t step; int n;
    if (role == 0) { src = wo + (size_t)(xc * 512 + wave * 64 + (lane >> 3)) * D + r * 64 + (lane & 7) * 8; step = (size_t)8 * D; n = 8; }
    else if (role == 1) { src = ((wave >> 2) ? wu : wg) + (size_t)((wave & 3) * 128 + (lane >> 1)) * F + (xc * 14 + (r - 8)) * 16 + (lane & 1) * 8; step = (size_t)32 * F; n = 4; }
    else { src = wd + (size_t)(wave * 224 + (lane >> 1)) * D + (xc * 4 + (r - 22)) * 16 + (lane & 1) * 8; step = (size_t)32 * D; n = 7; }
    const unsigned dst = img_l + wave * n * 1024;
    for (int i = 0; i < n; ++i) { glds16(src, dst + i * 1024); src += step; }
  }
  v4u bf[8];

  if (role == 0) {
    const int bb = wave >> 2, h = wave & 3;
    unsigned v[17];
    {
      gu64* g0 = GP + (size_t)((bb * 8 + xc) * 16) * 256 + h * 64 + lane;
      gu64* g1 = GS + (size_t)((bb * 8 + xc) * 16 + ((lane & 31) >> 1)) * 8 + h * 2 + (lane & 1);
      poll1(g1, tag);
      for (unsigned spins = 0;; ++spins) {
        bool ok = true;
        u64 xx[17];
#pragma unroll
        for (int k = 0; k < 16; ++k) xx[k] = __hip_atomic_load(g0 + k * 256, RLX_AGENT);
        xx[16] = __hip_atomic_load(g1, RLX_AGENT);
#pragma unroll
        for (int k = 0; k < 17; ++k) { v[k] = (unsigned)xx[k]; ok &= (unsigned)(xx[k] >> 32) == tag; }
        if (__all(ok) || spins > SPIN_CAP) break;
        __builtin_amdgcn_s_sleep(2);
      }
    }
    if (wave == 0) STAMP(5);
    {
      const float val = __uint_as_float(v[16]);
      const float swp = dpp<XOR1>(val);
      const float m = (lane & 1) ? swp : val, l = (lane & 1) ? val : swp;
      float M = m; M = fmaxf(M, dpp<XOR2>(M)); M = fmaxf(M, dpp<ROR4>(M)); M = fmaxf(M, dpp<ROR8>(M)); M = x16_max(M);
      const float wgt = l * __expf(m - M);
      float L = wgt; L += dpp<XOR2>(L); L += dpp<ROR4>(L); L += dpp<ROR8>(L); L = x16_sum(L);
      float* WT = (float*)(smem + L_WT + wave * 64);
      if (lane < 32 && !(lane & 1)) WT[lane >> 1] = wgt * __builtin_amdgcn_rcpf(L);
      LWAIT();
      float o0 = 0.f, o1 = 0.f;
#pragma unroll
      for (int q4 = 0; q4 < 4; ++q4) {
        const f32x4 w4 = *(const f32x4*)(WT + 4 * q4);
#pragma unroll
        for (int j = 0; j < 4; ++j) { o0 += w4[j] * lo(v[4 * q4 + j]); o1 += w4[j] * hi(v[4 * q4 + j]); }
      }
      *(unsigned*)(smem + L_OA + bb * 1024 + h * 256 + lane * 4) = pk2(o0, o1);
    }
    if (wave == 0) STAMP(16);
    asm volatile("s_waitcnt vmcnt(0)" ::: "memory");
    WBAR();
    if (wave == 0) STAMP(17);
    tr_frag8<4096, 512>(img_l + ((wave >> 2) * 256 + 8 * lg + (li >> 2)) * 128 + (wave & 3) * 32 + 8 * (li & 3), bf);
    if (wave == 0) STAMP(12);
    {
      const int kh = wave >> 2;
      const unsigned char* ab = smem + L_OA + (li < 2 ? li : 2) * 1024 + kh * 512 + lg * 16;
      f32x4 c = {0.f, 0.f, 0.f, 0.f};
#pragma unroll
      for (int ks = 0; ks < 8; ++ks) c = MFMA(*(const v4u*)(ab + ks * 64), bf[ks], c);
      if (lg == 0) { RED[wave * 64 + li] = c[0]; RED[wave * 64 + 16 + li] = c[1]; }
    }
    if (wave == 0) STAMP(18);
    WBAR();
    if (wave == 0) STAMP(19);
    if (tid < 128) {
      const int q = tid >> 6, col = tid & 63, nt = col >> 4, ci = col & 15;
      const float a = RED[nt * 64 + q * 16 + ci] + RED[(4 + nt) * 64 + q * 16 + ci];
      st_gran(GH + (xc * 2 + q) * 512 + r * 64 + col, tag, __float_as_uint(a));
    }
    if (wave == 0) STAMP(6);
    STAMP_FLUSH();
    return;
  }

  if (role == 1) {
    asm volatile("s_waitcnt vmcnt(0)" ::: "memory");
    WBAR();
    tr_frag8<1024, 128>(img_l + (wave & 1) * 16384 + ((wave >> 1) * 128 + 8 * lg + (li >> 2)) * 32 + 8 * (li & 3), bf);
    const int fb = xc * 14 + (r - 8);
    unsigned v[16];
    poll1(GH + (lane & 15) * 512 + wave * 64 + 63, tag);
    sweep<16>(GH + tid, 512, tag, v, 16);
    if (wave == 0) STAMP(5);
    if (lane < 32) ((v4u*)(aw + 1024))[lane] = v4u{0u, 0u, 0u, 0u};
    float s0 = 0.f, s1 = 0.f;
#pragma unroll
    for (int k = 0; k < 8; ++k) { s0 += __uint_as_float(v[2 * k]); s1 += __uint_as_float(v[2 * k + 1]); }
    {
      const unsigned sp = pk2(s0, s1);
      const unsigned hp = pk2(XS[tid] + lo(sp), XS[D + tid] + hi(sp));
      HS[tid] = lo(hp); HS[D + tid] = hi(hp);
    }
    if (wave == 0) STAMP(16);
    WBAR();
    if (wave == 0) STAMP(17);
    {
      float va[8], vb[8];
      const f32x4 a0 = *(const f32x4*)(HS + 8 * lane), a1 = *(const f32x4*)(HS + 8 * lane + 4);
      const f32x4 b0 = *(const f32x4*)(HS + D + 8 * lane), b1 = *(const f32x4*)(HS + D + 8 * lane + 4);
#pragma unroll
      for (int k = 0; k < 4; ++k) { va[k] = a0[k]; va[4 + k] = a1[k]; vb[k] = b0[k]; vb[4 + k] = b1[k]; }
      v4u pa, pb;
      ln2(va, vb, pa, pb);
      if (lg == (wave >> 1)) {
        *(v4u*)(aw + li * 16) = pa;
        *(v4u*)(aw + 512 + li * 16) = pb;
      }
      LWAIT();
      const unsigned char* ab = aw + (li < 2 ? li : 2) * 512 + lg * 16;
      f32x4 c = {0.f, 0.f, 0.f, 0.f};
#pragma unroll
      for (int ks = 0; ks < 4; ++ks) c = MFMA(*(const v4u*)(ab + ks * 64), bf[ks], c);
      if (lg == 0) { RED[wave * 64 + li] = c[0]; RED[wave * 64 + 16 + li] = c[1]; }
    }
    if (wave == 0) STAMP(18);
    WBAR();
    if (wave == 0) STAMP(19);
    if (tid < 16) {
      float g0 = 0.f, u0 = 0.f, g1 = 0.f, u1 = 0.f;
#pragma unroll
      for (int kq = 0; kq < 4; ++kq) {
        g0 += RED[(2 * kq) * 64 + tid]; u0 += RED[(2 * kq + 1) * 64 + tid];
        g1 += RED[(2 * kq) * 64 + 16 + tid]; u1 += RED[(2 * kq + 1) * 64 + 16 + tid];
      }
      const unsigned gp = pk2(g0, g1), up = pk2(u0, u1);
      const unsigned sp = pk2(1.0f / (1.0f + __expf(-lo(gp))), 1.0f / (1.0f + __expf(-hi(gp))));
      st_gran(GA + fb * 16 + tid, tag, pk2(lo(sp) * lo(up), hi(sp) * hi(up)));
    }
    if (wave == 0) STAMP(6);
    STAMP_FLUSH();
    return;
  }

  {
    asm volatile("s_waitcnt vmcnt(0)" ::: "memory");
    tr_frag8<1024, 128>(img_l + (wave * 224 + 8 * lg + (li >> 2)) * 32 + 8 * (li & 3), bf);
    const int cb = xc * 4 + (r - 22);
    float hres = 0.f;
    if (wave == 0) {
      unsigned v[8];
      const int q = (lane >> 4) & 1, c = lane & 15;
      sweep<8>(GH + q * 512 + cb * 16 + c, 1024, tag, v, 8);
      float a = 0.f;
#pragma unroll
      for (int k = 0; k < 8; ++k) a += __uint_as_float(v[k]);
      hres = rbf(XS[q * D + cb * 16 + c] + rbf(a));
    }
    {
      unsigned v[4];
      sweep<4>(GA + wave * 224 + lane, 64, tag, v, lane < 32 ? 4 : 3);
      if (lane < 32) ((v4u*)(aw + 1024))[lane] = v4u{0u, 0u, 0u, 0u};
      u16* A0 = (u16*)aw; u16* A1 = (u16*)(aw + 512);
#pragma unroll
      for (int j = 0; j < 4; ++j) if (j < 3 || lane < 32) { A0[j * 64 + lane] = (u16)(v[j] & 0xffffu); A1[j * 64 + lane] = (u16)(v[j] >> 16); }
    }
    if (wave == 0) STAMP(5);
    LWAIT();
    {
      const unsigned char* ab = aw + (li < 2 ? li : 2) * 512 + lg * 16;
      f32x4 c = {0.f, 0.f, 0.f, 0.f};
#pragma unroll
      for (int ks = 0; ks < 7; ++ks) c = MFMA(*(const v4u*)(ab + ks * 64), bf[ks], c);
      if (lg == 0) { RED[wave * 64 + li] = c[0]; RED[wave * 64 + 16 + li] = c[1]; }
    }
    WBAR();
    if (tid < 32) {
      float a = 0.f;
#pragma unroll
      for (int w = 0; w < NW; ++w) a += RED[w * 64 + tid];
      const int q = tid >> 4, c = tid & 15;
      out[q * D + cb * 16 + c] = (u16)(pk2(hres + rbf(a), 0.f) & 0xffffu);
      if (cb == 0 && tid == 0) __hip_atomic_store((gu32*)(ws + WS_EPOCH), tag, RLX_AGENT);
    }
    if (wave == 0) STAMP(6);
    STAMP_FLUSH();
  }
}

extern "C" void kernel_launch(void* const* d_in, const int* in_sizes, int n_in, void* d_out, int out_size, void* d_ws, size_t ws_size,
                              hipStream_t stream) {
  (void)hipFuncSetAttribute((const void*)mega, hipFuncAttributeMaxDynamicSharedMemorySize, L_END);
  mega<<<256, NT, L_END, stream>>>((const u16*)d_in[0], (const u16*)d_in[1], (const u16*)d_in[2], (const u16*)d_in[3], (const u16*)d_in[4],
                                   (const u16*)d_in[6], (const u16*)d_in[7], (const u16*)d_in[8], (u16*)d_out, (unsigned char*)d_ws);
}
```

```cpp
#include <hip/hip_runtime.h>
#include <stdint.h>

typedef unsigned short u16;
typedef unsigned long long u64;
typedef unsigned v4u __attribute__((ext_vector_type(4)));
typedef unsigned v2u __attribute__((ext_vector_type(2)));
typedef float f32x4 __attribute__((ext_vector_type(4)));
typedef short bf16x8 __attribute__((ext_vector_type(8)));
#define GAS __attribute__((address_space(1)))
#define LAS __attribute__((address_space(3)))
typedef GAS u64 gu64;
typedef GAS unsigned gu32;
#define RLX_AGENT __ATOMIC_RELAXED, __HIP_MEMORY_SCOPE_AGENT

constexpr int B = 2, D = 512, HQ = 32, HKV = 8, SK = 32768, DH = 128, W = 4096, F = 1792;
constexpr int NW = 8;
constexpr int NT = (NW + 1) * 64;
constexpr unsigned SPIN_CAP = 400000;
#define WQSIG 0
#define OPOLL 1
#define GPOLL 1

constexpr size_t WS_EPOCH = 0;
constexpr size_t WS_GQ = 4096;
constexpr size_t WS_GP = WS_GQ + 4096 * 8;
constexpr size_t WS_GS = WS_GP + 65536 * 8;
constexpr size_t WS_GH = WS_GS + 2048 * 8;
constexpr size_t WS_GA = WS_GH + 8192 * 8;
constexpr size_t WS_STAMP = 4u << 20;

constexpr int VROW = 288;
constexpr int WAREA = 32 * VROW;
constexpr int L_V = 0;
constexpr int L_IMG = L_V + NW * WAREA;
constexpr int L_WQ = L_IMG + 49152;
constexpr int L_XS = L_IMG + 65536;
constexpr int L_RED = L_XS + 4096;
constexpr int L_QS = L_RED + 2048;
constexpr int L_ML = L_QS + 5 * 256;
constexpr int L_OA = L_ML + NW * 4 * 2 * 4;
constexpr int L_XA = L_OA + 3 * 1024;
constexpr int L_WT = L_XA + 3 * 1024;
constexpr int L_PF = L_WT + NW * 64;
constexpr int L_HS = L_PF;
constexpr int L_SYNC = L_PF + NW * 1024;
constexpr int L_END = L_SYNC + 64;
constexpr int S_TAG = 0, S_QRDY = 1, S_VISS = 2, S_WRDY = 3, S_QPD = 4, S_WBAR = 5;
static_assert(L_END <= 160 * 1024, "LDS");

__device__ __forceinline__ float bf2f(unsigned x) { return __uint_as_float(x << 16); }
__device__ __forceinline__ float lo(unsigned w) { return __uint_as_float(w << 16); }
__device__ __forceinline__ float hi(unsigned w) { return __uint_as_float(w & 0xffff0000u); }
typedef __bf16 bf16x2_t __attribute__((ext_vector_type(2)));
typedef float f32x2_t __attribute__((ext_vector_type(2)));
__device__ __forceinline__ unsigned pk2(float a, float b) { return __builtin_bit_cast(unsigned, __builtin_convertvector((f32x2_t{a, b}), bf16x2_t)); }
__device__ __forceinline__ float rbf(float f) { return lo(pk2(f, f)); }

template <int CTRL> __device__ __forceinline__ float dpp(float x) {
  return __builtin_bit_cast(float, __builtin_amdgcn_mov_dpp(__builtin_bit_cast(int, x), CTRL, 0xf, 0xf, true));
}
constexpr int XOR1 = 0xB1, XOR2 = 0x4E, ROR4 = 0x124, ROR8 = 0x128;
__device__ __forceinline__ float x16_sum(float x) {
  auto s = __builtin_amdgcn_permlane16_swap(__float_as_uint(x), __float_as_uint(x), false, false);
  return __uint_as_float(s[0]) + __uint_as_float(s[1]);
}
__device__ __forceinline__ float x32_sum(float x) {
  auto t = __builtin_amdgcn_permlane32_swap(__float_as_uint(x), __float_as_uint(x), false, false);
  return __uint_as_float(t[0]) + __uint_as_float(t[1]);
}
__device__ __forceinline__ float x16_max(float x) {
  auto s = __builtin_amdgcn_permlane16_swap(__float_as_uint(x), __float_as_uint(x), false, false);
  return fmaxf(__uint_as_float(s[0]), __uint_as_float(s[1]));
}
__device__ __forceinline__ float x32_max(float x) {
  auto s = __builtin_amdgcn_permlane32_swap(__float_as_uint(x), __float_as_uint(x), false, false);
  return fmaxf(__uint_as_float(s[0]), __uint_as_float(s[1]));
}
__device__ __forceinline__ float row_sum(float v) { v += dpp<XOR1>(v); v += dpp<XOR2>(v); v += dpp<ROR4>(v); v += dpp<ROR8>(v); return v; }
__device__ __forceinline__ float wave_sum(float v) { return x32_sum(x16_sum(row_sum(v))); }

#define LWAIT() asm volatile("s_waitcnt lgkmcnt(0)" ::: "memory")
#define STAMP(i) do { } while (0)
#define STAMP_FLUSH() do { } while (0)
#define STAMP_DECL do { } while (0)

__device__ __forceinline__ void l_arrive(LAS unsigned* p, int lane) {
  LWAIT();
  if (lane == 0) __hip_atomic_fetch_add(p, 1u, __ATOMIC_RELAXED, __HIP_MEMORY_SCOPE_WORKGROUP);
}
__device__ __forceinline__ void l_wait(LAS unsigned* p, unsigned target) {
  for (unsigned spins = 0; __hip_atomic_load(p, __ATOMIC_RELAXED, __HIP_MEMORY_SCOPE_WORKGROUP) < target && spins < (1u << 24); ++spins) __builtin_amdgcn_s_sleep(1);
  asm volatile("" ::: "memory");
}

__device__ __forceinline__ v4u ldnt(const u16* p) { return __builtin_nontemporal_load((const v4u*)p); }
__device__ __forceinline__ void st_gran(gu64* g, unsigned tag, unsigned v) { __hip_atomic_store(g, ((u64)tag << 32) | v, RLX_AGENT); }

template <int N> __device__ __forceinline__ void sweep(gu64* g, int S, unsigned tag, unsigned (&v)[N], int nact) {
  for (unsigned spins = 0;; ++spins) {
    bool ok = true;
    u64 x[N];
#pragma unroll
    for (int k = 0; k < N; ++k) x[k] = __hip_atomic_load(g + (size_t)(k < nact ? k : 0) * S, RLX_AGENT);
#pragma unroll
    for (int k = 0; k < N; ++k) { v[k] = (unsigned)x[k]; ok &= (unsigned)(x[k] >> 32) == tag; }
    if (__all(ok) || spins > SPIN_CAP) return;
    __builtin_amdgcn_s_sleep(2);
  }
}

typedef __amdgpu_buffer_rsrc_t rsrc_t;
__device__ __forceinline__ v4u ld16_sc1(rsrc_t rs, unsigned off) { return __builtin_bit_cast(v4u, __builtin_amdgcn_raw_buffer_load_b128(rs, off, 0, 16)); }
__device__ __forceinline__ void st16_sc1(rsrc_t rs, unsigned off, v4u v) { __builtin_amdgcn_raw_buffer_store_b128(__builtin_bit_cast(__attribute__((ext_vector_type(4))) int, v), rs, off, 0, 16); }
__device__ __forceinline__ void poll1(gu64* g, unsigned tag) {
  for (unsigned spins = 0;; ++spins) {
    const u64 x = __hip_atomic_load(g, RLX_AGENT);
    if (__all((unsigned)(x >> 32) == tag) || spins > SPIN_CAP) return;
    __builtin_amdgcn_s_sleep(2);
  }
}
__device__ __forceinline__ void ln2(const float (&va)[8], const float (&vb)[8], v4u& pa, v4u& pb) {
  float s0 = ((va[0] + va[1]) + (va[2] + va[3])) + ((va[4] + va[5]) + (va[6] + va[7]));
  float s1 = ((vb[0] + vb[1]) + (vb[2] + vb[3])) + ((vb[4] + vb[5]) + (vb[6] + vb[7]));
  s0 = wave_sum(s0); s1 = wave_sum(s1);
  const float m0 = s0 * (1.0f / D), m1 = s1 * (1.0f / D);
  float q0 = 0.f, q1 = 0.f;
#pragma unroll
  for (int i = 0; i < 8; ++i) { const float d0 = va[i] - m0, d1 = vb[i] - m1; q0 = fmaf(d0, d0, q0); q1 = fmaf(d1, d1, q1); }
  q0 = wave_sum(q0); q1 = wave_sum(q1);
  const unsigned mv = pk2(m0, m1), vv = pk2(q0 * (1.0f / D), q1 * (1.0f / D));
  const unsigned ve = pk2(lo(vv) + 1e-5f, hi(vv) + 1e-5f);
  const unsigned sd = pk2(__builtin_sqrtf(lo(ve)), __builtin_sqrtf(hi(ve)));
  const float r0 = 1.0f / lo(sd), r1 = 1.0f / hi(sd), mb0 = lo(mv), mb1 = hi(mv);
#pragma unroll
  for (int i = 0; i < 4; ++i) {
    const unsigned da = pk2(va[2 * i] - mb0, va[2 * i + 1] - mb0), db = pk2(vb[2 * i] - mb1, vb[2 * i + 1] - mb1);
    pa[i] = pk2(lo(da) * r0, hi(da) * r0);
    pb[i] = pk2(lo(db) * r1, hi(db) * r1);
  }
}

template <int OFF0, int OFF1> __device__ __forceinline__ void tr_issue(unsigned addr, v2u& a, v2u& b) {
  asm volatile("ds_read_b64_tr_b16 %0, %2 offset:%c3\n\tds_read_b64_tr_b16 %1, %2 offset:%c4" : "=&v"(a), "=&v"(b) : "v"(addr), "i"(OFF0), "i"(OFF1) : "memory");
}
#define PIN8(t, o) asm volatile("" : "+v"(t[o + 0]), "+v"(t[o + 1]), "+v"(t[o + 2]), "+v"(t[o + 3]), "+v"(t[o + 4]), "+v"(t[o + 5]), "+v"(t[o + 6]), "+v"(t[o + 7]))
#define MFMA(a, b, c) __builtin_amdgcn_mfma_f32_16x16x32_bf16(__builtin_bit_cast(bf16x8, a), __builtin_bit_cast(bf16x8, b), c, 0, 0, 0)
template <int KS, int H> __device__ __forceinline__ void tr_frag8(unsigned addr, v4u (&f)[8]) {
  v2u t[16];
  tr_issue<0 * KS, 0 * KS + H>(addr, t[0], t[1]); tr_issue<1 * KS, 1 * KS + H>(addr, t[2], t[3]);
  tr_issue<2 * KS, 2 * KS + H>(addr, t[4], t[5]); tr_issue<3 * KS, 3 * KS + H>(addr, t[6], t[7]);
  tr_issue<4 * KS, 4 * KS + H>(addr, t[8], t[9]); tr_issue<5 * KS, 5 * KS + H>(addr, t[10], t[11]);
  tr_issue<6 * KS, 6 * KS + H>(addr, t[12], t[13]); tr_issue<7 * KS, 7 * KS + H>(addr, t[14], t[15]);
  LWAIT();
  PIN8(t, 0); PIN8(t, 8);
#pragma unroll
  for (int c = 0; c < 8; ++c) f[c] = v4u{t[2 * c].x, t[2 * c].y, t[2 * c + 1].x, t[2 * c + 1].y};
}
__device__ __forceinline__ void glds16(const u16* gsrc, unsigned lds_dst) {
  unsigned keep;
  asm volatile("s_mov_b32 %0, m0\n\ts_mov_b32 m0, %2\n\ts_nop 0\n\tglobal_load_lds_dwordx4 %1, off nt\n\ts_mov_b32 m0, %0" : "=&s"(keep) : "v"(gsrc), "s"(lds_dst) : "memory");
}

__global__ void __launch_bounds__(NT, 3) mega(const u16* __restrict__ x, const u16* __restrict__ kc, const u16* __restrict__ vc,
                                              const u16* __restrict__ wq, const u16* __restrict__ wo, const u16* __restrict__ wg,
                                              const u16* __restrict__ wu, const u16* __restrict__ wd, u16* __restrict__ out, unsigned char* ws_) {
  extern __shared__ __attribute__((aligned(16))) unsigned char smem[];
  GAS unsigned char* ws = (GAS unsigned char*)ws_;
  const int lane = threadIdx.x & 63, wave0 = __builtin_amdgcn_readfirstlane(threadIdx.x >> 6);
  const int xc = blockIdx.x & 7, r = blockIdx.x >> 3;
  const int kvh = xc, b = r >> 4, s = r & 15;
  const int cbk = kvh * 32 + r;
  const int role = r < 8 ? 0 : r < 22 ? 1 : r < 26 ? 2 : 3;
  const int li = lane & 15, lg = lane >> 4;
  float* XS = (float*)(smem + L_XS); float* HS = (float*)(smem + L_HS);
  float* RED = (float*)(smem + L_RED); float* ML = (float*)(smem + L_ML);
  LAS unsigned* SY = (LAS unsigned*)(smem + L_SYNC);
  const unsigned img_l = (unsigned)(size_t)(LAS unsigned char*)(smem + L_IMG);
  gu64* GQ = (gu64*)(ws + WS_GQ); gu64* GP = (gu64*)(ws + WS_GP); gu64* GS = (gu64*)(ws + WS_GS);
  gu64* GH = (gu64*)(ws + WS_GH); gu64* GA = (gu64*)(ws + WS_GA);
  STAMP_DECL;

  if (wave0 == 0) {
    __builtin_amdgcn_s_setprio(3);
    STAMP(0);
    const v4u xa = *(const v4u*)(x + 8 * lane), xb = *(const v4u*)(x + D + 8 * lane);
    v4u wqr[16];
#pragma unroll
    for (int i = 0; i < 16; ++i) wqr[i] = ldnt(wq + (size_t)((lane >> 1) + 32 * i) * (HQ * DH) + cbk * 16 + (lane & 1) * 8);
    const unsigned ep = __hip_atomic_load((gu32*)(ws + WS_EPOCH), RLX_AGENT);
    __builtin_amdgcn_sched_barrier(0);
    if (lane < 16) SY[lane] = 0u;
    LWAIT();
    __builtin_amdgcn_s_barrier();
    asm volatile("" ::: "memory");
    ((unsigned*)(smem + L_QS + 4 * 256))[lane] = 0u;
    ((v4u*)(smem + L_XA + 2048))[lane] = v4u{0u, 0u, 0u, 0u};
    ((v4u*)(smem + L_OA + 2048))[lane] = v4u{0u, 0u, 0u, 0u};
    {
      float va[8], vb[8];
#pragma unroll
      for (int k = 0; k < 4; ++k) { va[2 * k] = lo(xa[k]); va[2 * k + 1] = hi(xa[k]); vb[2 * k] = lo(xb[k]); vb[2 * k + 1] = hi(xb[k]); }
      *(f32x4*)(XS + 8 * lane) = f32x4{va[0], va[1], va[2], va[3]}; *(f32x4*)(XS + 8 * lane + 4) = f32x4{va[4], va[5], va[6], va[7]};
      *(f32x4*)(XS + D + 8 * lane) = f32x4{vb[0], vb[1], vb[2], vb[3]}; *(f32x4*)(XS + D + 8 * lane + 4) = f32x4{vb[4], vb[5], vb[6], vb[7]};
      v4u pa, pb;
      ln2(va, vb, pa, pb);
      *(v4u*)(smem + L_XA + lane * 16) = pa;
      *(v4u*)(smem + L_XA + 1024 + lane * 16) = pb;
    }
    STAMP(7);
    f32x4 c = {0.f, 0.f, 0.f, 0.f};
    {
      unsigned char* wl = smem + L_WQ;
      const unsigned tr32 = (unsigned)(size_t)(LAS unsigned char*)wl + (8 * lg + (li >> 2)) * 32 + 8 * (li & 3);
      const unsigned char* ab = smem + L_XA + (li < 2 ? li : 2) * 1024 + lg * 16;
#pragma unroll
      for (int hq = 0; hq < 2; ++hq) {
#pragma unroll
        for (int i = 0; i < 8; ++i) *(v4u*)(wl + (32 * (8 * hq + i) + (lane >> 1)) * 32 + (lane & 1) * 16) = wqr[8 * hq + i];
        LWAIT();
        if (hq == WQSIG && lane == 0) SY[S_VISS] = 1u;
        v4u f[8], af[8];
#pragma unroll
        for (int i = 0; i < 8; ++i) af[i] = *(const v4u*)(ab + (8 * hq + i) * 64);
        tr_frag8<1024, 128>(tr32 + hq * 8192, f);
#pragma unroll
        for (int i = 0; i < 8; ++i) c = MFMA(af[i], f[i], c);
      }
    }
    const unsigned tag = __builtin_amdgcn_readfirstlane(ep) + 1u;
    if (lane < 16) st_gran(GQ + cbk * 16 + lane, tag, pk2(c[0], c[1]));
    if (lane == 0) { SY[S_TAG] = tag; SY[S_QPD] = 1u; }
    STAMP(1);
    {
      unsigned v[8];
      sweep<8>(GQ + kvh * 512 + lane, 64, tag, v, 8);
      u16* QS = (u16*)(smem + L_QS);
#pragma unroll
      for (int k = 0; k < 8; ++k) QS[k * 64 + lane] = (u16)(b ? (v[k] >> 16) : (v[k] & 0xffffu));
    }
    l_arrive(SY + S_QRDY, lane);
    STAMP(2);
    STAMP_FLUSH();
    return;
  }

  __builtin_amdgcn_s_barrier();
  asm volatile("" ::: "memory");

  const int wave = wave0 - 1, tid = threadIdx.x - 64;
  unsigned char* wa = smem + L_V + wave * WAREA;
  const unsigned wa_l = (unsigned)(size_t)(LAS unsigned char*)wa;
  unsigned char* aw = wa + 4096;
#define WBAR() do { LWAIT(); __builtin_amdgcn_s_barrier(); asm volatile("" ::: "memory"); } while (0)
  const rsrc_t rs = __builtin_amdgcn_make_buffer_rsrc((void*)ws_, 0, 0x7fffffff, 0x00020000);
  v4u K[8], V[8];
  const size_t kvbase = ((size_t)(b * HKV + kvh) * SK + (SK - W) + s * 256 + wave * 32) * DH + (size_t)lg * DH + li * 8;
  l_wait(SY + S_VISS, 1u);
#pragma unroll
  for (int j = 0; j < 8; ++j) K[j] = ldnt(kc + kvbase + (size_t)4 * j * DH);
#pragma unroll
  for (int j = 0; j < 8; ++j) V[j] = ldnt(vc + kvbase + (size_t)4 * j * DH);
  asm volatile("" ::: "memory");
  v4u kf[8];
  {
#pragma unroll
    for (int j = 0; j < 8; ++j) *(v4u*)(wa + (4 * j + lg) * VROW + li * 16) = K[j];
    LWAIT();
#pragma unroll
    for (int t = 0; t < 2; ++t)
#pragma unroll
      for (int kk = 0; kk < 4; ++kk) kf[t * 4 + kk] = *(const v4u*)(wa + (t * 16 + li) * VROW + kk * 64 + lg * 16);
    LWAIT();
    asm volatile("" : "+v"(kf[0]), "+v"(kf[1]), "+v"(kf[2]), "+v"(kf[3]), "+v"(kf[4]), "+v"(kf[5]), "+v"(kf[6]), "+v"(kf[7]));
  }
  l_wait(SY + S_QRDY, 1u);
  if (wave == 0) STAMP(13);
  const unsigned tag = __builtin_amdgcn_readfirstlane(SY[S_TAG]);
  {
    f32x4 c[2];
    v4u qb[4];
#pragma unroll
    for (int kk = 0; kk < 4; ++kk) qb[kk] = *(const v4u*)(smem + L_QS + (li < 4 ? li : 4) * 256 + kk * 64 + lg * 16);
#pragma unroll
    for (int t = 0; t < 2; ++t) {
      c[t] = f32x4{0.f, 0.f, 0.f, 0.f};
#pragma unroll
      for (int kk = 0; kk < 4; ++kk) c[t] = MFMA(kf[t * 4 + kk], qb[kk], c[t]);
    }
    float sc[8];
    constexpr float SCL = 0.08838834764831845f;
#pragma unroll
    for (int t = 0; t < 2; ++t)
#pragma unroll
      for (int p = 0; p < 2; ++p) {
        const unsigned a = pk2(c[t][2 * p], c[t][2 * p + 1]);
        const unsigned bq = pk2(lo(a) * SCL, hi(a) * SCL);
        sc[4 * t + 2 * p] = lo(bq); sc[4 * t + 2 * p + 1] = hi(bq);
      }
    float mx = fmaxf(fmaxf(fmaxf(sc[0], sc[1]), fmaxf(sc[2], sc[3])), fmaxf(fmaxf(sc[4], sc[5]), fmaxf(sc[6], sc[7])));
    mx = x32_max(x16_max(mx));
    const float M = mx;
    v4u pf;
    float l = 0.f;
#pragma unroll
    for (int p = 0; p < 4; ++p) {
      const unsigned pp = pk2(__expf(sc[2 * p] - M), __expf(sc[2 * p + 1] - M));
      l += lo(pp) + hi(pp);
      pf[p] = li < 4 ? pp : 0u;
    }
    l = x32_sum(x16_sum(l));
    if (lane < 4) *(float2*)(ML + (wave * 4 + lane) * 2) = make_float2(mx, l);
    *(v4u*)(smem + L_PF + wave * 1024 + lane * 16) = pf;
#pragma unroll
    for (int j = 0; j < 8; ++j) *(v4u*)(wa + (4 * j + lg) * VROW + li * 16) = V[j];
    if (wave == 0) STAMP(11);
    if (wave == 7) STAMP(15);
  if (role == 0) {
    const u16* src; size_t step; int n;
    if (role == 0) { src = wo + (size_t)(xc * 512 + wave * 64 + (lane >> 3)) * D + r * 64 + (lane & 7) * 8; step = (size_t)8 * D; n = 8; }
    else if (role == 1) { src = ((wave >> 2) ? wu : wg) + (size_t)((wave & 3) * 128 + (lane >> 1)) * F + (xc * 14 + (r - 8)) * 16 + (lane & 1) * 8; step = (size_t)32 * F; n = 4; }
    else { src = wd + (size_t)(wave * 224 + (lane >> 1)) * D + (xc * 4 + (r - 22)) * 16 + (lane & 1) * 8; step = (size_t)32 * D; n = 7; }
    const unsigned dst = img_l + wave * n * 1024;
    for (int i = 0; i < n; ++i) { glds16(src, dst + i * 1024); src += step; }
  }
    WBAR();
    if (wave == 0) STAMP(3);
    const unsigned vb0 = (unsigned)(size_t)(LAS unsigned char*)(smem + L_V) + (4 * lg + (li >> 2)) * VROW + 8 * (li & 3) + 32 * wave;
    v2u t[16];
    tr_issue<0 * WAREA, 0 * WAREA + 16 * VROW>(vb0, t[0], t[1]); tr_issue<1 * WAREA, 1 * WAREA + 16 * VROW>(vb0, t[2], t[3]);
    tr_issue<2 * WAREA, 2 * WAREA + 16 * VROW>(vb0, t[4], t[5]); tr_issue<3 * WAREA, 3 * WAREA + 16 * VROW>(vb0, t[6], t[7]);
    tr_issue<0 * WAREA, 0 * WAREA + 16 * VROW>(vb0 + 4 * WAREA, t[8], t[9]); tr_issue<1 * WAREA, 1 * WAREA + 16 * VROW>(vb0 + 4 * WAREA, t[10], t[11]);
    tr_issue<2 * WAREA, 2 * WAREA + 16 * VROW>(vb0 + 4 * WAREA, t[12], t[13]); tr_issue<3 * WAREA, 3 * WAREA + 16 * VROW>(vb0 + 4 * WAREA, t[14], t[15]);
    v4u pfr[8];
#pragma unroll
    for (int kb = 0; kb < 8; ++kb) pfr[kb] = *(const v4u*)(smem + L_PF + kb * 1024 + lane * 16);
    float mw[NW], lw[NW];
#pragma unroll
    for (int w = 0; w < NW; ++w) { const float2 a = *(const float2*)(ML + (w * 4 + (li & 3)) * 2); mw[w] = a.x; lw[w] = a.y; }
    const float MG = fmaxf(fmaxf(fmaxf(mw[0], mw[1]), fmaxf(mw[2], mw[3])), fmaxf(fmaxf(mw[4], mw[5]), fmaxf(mw[6], mw[7])));
    float L = 0.f;
#pragma unroll
    for (int w = 0; w < NW; ++w) { mw[w] = __expf(mw[w] - MG); L = fmaf(mw[w], lw[w], L); }
    LWAIT();
    PIN8(t, 0); PIN8(t, 8);
    f32x4 o = {0.f, 0.f, 0.f, 0.f};
#pragma unroll
    for (int kb = 0; kb < 8; ++kb) {
      const f32x4 ob = MFMA((v4u{t[2 * kb].x, t[2 * kb].y, t[2 * kb + 1].x, t[2 * kb + 1].y}), pfr[kb], (f32x4{0.f, 0.f, 0.f, 0.f}));
#pragma unroll
      for (int e = 0; e < 4; ++e) o[e] = fmaf(mw[kb], ob[e], o[e]);
    }
    const float inv = __builtin_amdgcn_rcpf(L);
    const int slot = (b * 8 + kvh) * 16 + s;
    if (li < 4) {
      st16_sc1(rs, (unsigned)(WS_GP + (size_t)slot * 2048 + wave * 256 + (li * 4 + lg) * 16), v4u{pk2(o[0] * inv, o[1] * inv), tag, pk2(o[2] * inv, o[3] * inv), tag});
      if (wave == 0 && lg == 0) st16_sc1(rs, (unsigned)(WS_GS + (size_t)slot * 64 + li * 16), v4u{__float_as_uint(MG), tag, __float_as_uint(L), tag});
    }
  }
  if (wave == 0) STAMP(4);
  if (role == 3) { STAMP_FLUSH(); return; }

  if (role == 1 || role == 2) {
    const u16* src; size_t step; int n;
    if (role == 0) { src = wo + (size_t)(xc * 512 + wave * 64 + (lane >> 3)) * D + r * 64 + (lane & 7) * 8; step = (size_t)8 * D; n = 8; }
    else if (role == 1) { src = ((wave >> 2) ? wu : wg) + (size_t)((wave & 3) * 128 + (lane >> 1)) * F + (xc * 14 + (r - 8)) * 16 + (lane & 1) * 8; step = (size_t)32 * F; n = 4; }
    else { src = wd + (size_t)(wave * 224 + (lane >> 1)) * D + (xc * 4 + (r - 22)) * 16 + (lane & 1) * 8; step = (size_t)32 * D; n = 7; }
    const unsigned dst = img_l + wave * n * 1024;
    for (int i = 0; i < n; ++i) { glds16(src, dst + i * 1024); src += step; }
  }
  v4u bf[8];

  if (role == 0) {
    const int bb = wave >> 2, h = wave & 3;
    const unsigned aoff = (unsigned)(WS_GP + (size_t)((bb * 8 + xc) * 16 + li) * 2048 + (lg >> 1) * 256 + (h * 4 + 2 * (lg & 1)) * 16);
    const unsigned soff = (unsigned)(WS_GS + ((size_t)(((bb * 8 + xc) * 16 + 4 * lg) * 8 + h * 2)) * 8);
    if (OPOLL) {
      gu64* g = GP + (size_t)((bb * 8 + xc) * 16 + li) * 256 + 2 * lg * 32 + (h * 4 + 3) * 2 + 1;
      for (unsigned spins = 0;; ++spins) {
        const u64 x0 = __hip_atomic_load(g, RLX_AGENT), x1 = __hip_atomic_load(g + 32, RLX_AGENT);
        if (__all(((unsigned)(x0 >> 32) == tag) & ((unsigned)(x1 >> 32) == tag)) || spins > SPIN_CAP) break;
        __builtin_amdgcn_s_sleep(2);
      }
    }
    if (wave == 0) STAMP(16);
    if (wave == 7) STAMP(17);
    if (wave == 3) STAMP(20);
    v4u ga[8], gs[4];
#pragma unroll
    for (int ks = 0; ks < 4; ++ks) { ga[2 * ks] = ld16_sc1(rs, aoff + ks * 512); ga[2 * ks + 1] = ld16_sc1(rs, aoff + ks * 512 + 16); }
#pragma unroll
    for (int q = 0; q < 4; ++q) gs[q] = ld16_sc1(rs, soff + q * 64);
    asm volatile("s_waitcnt vmcnt(12)" ::: "memory");
    WBAR();
    if (wave == 0) STAMP(12);
    for (unsigned spins = 0;; ++spins) {
      bool ok = true;
#pragma unroll
      for (int k = 0; k < 8; ++k) ok &= (ga[k][1] == tag) & (ga[k][3] == tag);
#pragma unroll
      for (int q = 0; q < 4; ++q) ok &= (gs[q][1] == tag) & (gs[q][3] == tag);
      if (__all(ok) || spins > SPIN_CAP) break;
      __builtin_amdgcn_s_sleep(2);
#pragma unroll
      for (int ks = 0; ks < 4; ++ks) { ga[2 * ks] = ld16_sc1(rs, aoff + ks * 512); ga[2 * ks + 1] = ld16_sc1(rs, aoff + ks * 512 + 16); }
#pragma unroll
      for (int q = 0; q < 4; ++q) gs[q] = ld16_sc1(rs, soff + q * 64);
    }
    if (wave == 0) STAMP(5);
    f32x4 c[4];
#pragma unroll
    for (int nt = 0; nt < 4; ++nt) c[nt] = f32x4{0.f, 0.f, 0.f, 0.f};
    {
      const unsigned ba = img_l + (h * 128 + 8 * lg + (li >> 2)) * 128 + 8 * (li & 3);
      v2u t[2][8];
      tr_issue<0, 512>(ba, t[0][0], t[0][1]); tr_issue<32, 32 + 512>(ba, t[0][2], t[0][3]);
      tr_issue<64, 64 + 512>(ba, t[0][4], t[0][5]); tr_issue<96, 96 + 512>(ba, t[0][6], t[0][7]);
#pragma unroll
      for (int ks = 0; ks < 4; ++ks) {
        LWAIT(); PIN8(t[ks & 1], 0);
        if (ks < 3) {
          const unsigned bk = ba + (ks + 1) * 4096;
          tr_issue<0, 512>(bk, t[(ks + 1) & 1][0], t[(ks + 1) & 1][1]); tr_issue<32, 32 + 512>(bk, t[(ks + 1) & 1][2], t[(ks + 1) & 1][3]);
          tr_issue<64, 64 + 512>(bk, t[(ks + 1) & 1][4], t[(ks + 1) & 1][5]); tr_issue<96, 96 + 512>(bk, t[(ks + 1) & 1][6], t[(ks + 1) & 1][7]);
        }
        const v4u af = v4u{ga[2 * ks][0], ga[2 * ks][2], ga[2 * ks + 1][0], ga[2 * ks + 1][2]};
#pragma unroll
        for (int nt = 0; nt < 4; ++nt)
          c[nt] = MFMA(af, (v4u{t[ks & 1][2 * nt].x, t[ks & 1][2 * nt].y, t[ks & 1][2 * nt + 1].x, t[ks & 1][2 * nt + 1].y}), c[nt]);
      }
    }
    float w4[4];
    {
      float M = fmaxf(fmaxf(__uint_as_float(gs[0][0]), __uint_as_float(gs[1][0])), fmaxf(__uint_as_float(gs[2][0]), __uint_as_float(gs[3][0])));
      M = x32_max(x16_max(M));
      float L = 0.f;
#pragma unroll
      for (int q = 0; q < 4; ++q) { w4[q] = __uint_as_float(gs[q][2]) * __expf(__uint_as_float(gs[q][0]) - M); L += w4[q]; }
      L = x32_sum(x16_sum(L));
      const float inv = __builtin_amdgcn_rcpf(L);
#pragma unroll
      for (int q = 0; q < 4; ++q) w4[q] *= inv;
    }
#pragma unroll
    for (int nt = 0; nt < 4; ++nt) {
      float a2 = (w4[0] * c[nt][0] + w4[1] * c[nt][1]) + (w4[2] * c[nt][2] + w4[3] * c[nt][3]);
      a2 = x32_sum(x16_sum(a2));
      if (lg == 0) RED[wave * 64 + nt * 16 + li] = a2;
    }
    if (wave == 0) STAMP(18);
    WBAR();
    if (wave == 0) STAMP(19);
    if (tid < 64) {
      const float a0 = (RED[tid] + RED[64 + tid]) + (RED[128 + tid] + RED[192 + tid]);
      const float a1 = (RED[256 + tid] + RED[320 + tid]) + (RED[384 + tid] + RED[448 + tid]);
      st_gran(GH + xc * 512 + r * 64 + tid, tag, pk2(a0, a1));
    }
    if (wave == 0) STAMP(6);
    STAMP_FLUSH();
    return;
  }

  if (role == 1) {
    asm volatile("s_waitcnt vmcnt(0)" ::: "memory");
    WBAR();
    tr_frag8<1024, 128>(img_l + (wave & 1) * 16384 + ((wave >> 1) * 128 + 8 * lg + (li >> 2)) * 32 + 8 * (li & 3), bf);
    const int fb = xc * 14 + (r - 8);
    if (GPOLL) poll1(GH + (lane & 7) * 512 + wave * 64 + 63, tag);
    v4u gh[4];
    {
      const unsigned off = (unsigned)(WS_GH + ((size_t)((lane >> 5) * 4 * 512 + wave * 64 + 2 * (lane & 31))) * 8);
      for (unsigned spins = 0;; ++spins) {
#pragma unroll
        for (int k = 0; k < 4; ++k) gh[k] = ld16_sc1(rs, off + k * 4096);
        bool ok = true;
#pragma unroll
        for (int k = 0; k < 4; ++k) ok &= (gh[k][1] == tag) & (gh[k][3] == tag);
        if (__all(ok) || spins > SPIN_CAP) break;
        __builtin_amdgcn_s_sleep(2);
      }
    }
    if (wave == 0) STAMP(5);
    if (lane < 32) ((v4u*)(aw + 1024))[lane] = v4u{0u, 0u, 0u, 0u};
    {
      const int col = wave * 64 + 2 * (lane & 31);
      const float2 xa2 = *(const float2*)(XS + col), xb2 = *(const float2*)(XS + D + col);
      const float p00 = x32_sum((lo(gh[0][0]) + lo(gh[1][0])) + (lo(gh[2][0]) + lo(gh[3][0]))), p01 = x32_sum((hi(gh[0][0]) + hi(gh[1][0])) + (hi(gh[2][0]) + hi(gh[3][0])));
      const float p10 = x32_sum((lo(gh[0][2]) + lo(gh[1][2])) + (lo(gh[2][2]) + lo(gh[3][2]))), p11 = x32_sum((hi(gh[0][2]) + hi(gh[1][2])) + (hi(gh[2][2]) + hi(gh[3][2])));
      const unsigned s0 = pk2(p00, p10), s1 = pk2(p01, p11);
      const unsigned h0 = pk2(xa2.x + lo(s0), xa2.y + hi(s0)), h1 = pk2(xb2.x + lo(s1), xb2.y + hi(s1));
      if (lane < 32) { ((unsigned*)(smem + L_HS))[(col >> 1)] = h0; ((unsigned*)(smem + L_HS + 1024))[(col >> 1)] = h1; }
      const float ma = x16_sum(row_sum(lo(h0) + hi(h0))) * (1.0f / 64), mb = x16_sum(row_sum(lo(h1) + hi(h1))) * (1.0f / 64);
      const float da0 = lo(h0) - ma, da1 = hi(h0) - ma, db0 = lo(h1) - mb, db1 = hi(h1) - mb;
      const float qa = x16_sum(row_sum(da0 * da0 + da1 * da1)), qb = x16_sum(row_sum(db0 * db0 + db1 * db1));
      if (lane == 0) *(f32x4*)(smem + L_HS + 2048 + wave * 16) = f32x4{ma, qa, mb, qb};
    }
    if (wave == 0) STAMP(16);
    WBAR();
    if (wave == 0) STAMP(17);
    {
      float m0 = 0.f, m1 = 0.f, q0 = 0.f, q1 = 0.f;
      f32x4 st[NW];
#pragma unroll
      for (int w = 0; w < NW; ++w) { st[w] = *(const f32x4*)(smem + L_HS + 2048 + w * 16); m0 += st[w][0]; m1 += st[w][2]; }
      m0 *= (1.0f / NW); m1 *= (1.0f / NW);
#pragma unroll
      for (int w = 0; w < NW; ++w) { const float e0 = st[w][0] - m0, e1 = st[w][2] - m1; q0 += st[w][1] + 64.f * e0 * e0; q1 += st[w][3] + 64.f * e1 * e1; }
      const unsigned mv = pk2(m0, m1), vv = pk2(q0 * (1.0f / D), q1 * (1.0f / D));
      const unsigned ve = pk2(lo(vv) + 1e-5f, hi(vv) + 1e-5f);
      const unsigned sd = pk2(__builtin_sqrtf(lo(ve)), __builtin_sqrtf(hi(ve)));
      const float r0 = 1.0f / lo(sd), r1 = 1.0f / hi(sd);
      const unsigned h0 = ((const unsigned*)(smem + L_HS))[(wave >> 1) * 64 + lane], h1 = ((const unsigned*)(smem + L_HS + 1024))[(wave >> 1) * 64 + lane];
      const unsigned d0 = pk2(lo(h0) - lo(mv), hi(h0) - lo(mv)), d1 = pk2(lo(h1) - hi(mv), hi(h1) - hi(mv));
      ((unsigned*)aw)[lane] = pk2(lo(d0) * r0, hi(d0) * r0);
      ((unsigned*)(aw + 512))[lane] = pk2(lo(d1) * r1, hi(d1) * r1);
      LWAIT();
      const unsigned char* ab = aw + (li < 2 ? li : 2) * 512 + lg * 16;
      f32x4 c = {0.f, 0.f, 0.f, 0.f};
#pragma unroll
      for (int ks = 0; ks < 4; ++ks) c = MFMA(*(const v4u*)(ab + ks * 64), bf[ks], c);
      if (lg == 0) { RED[wave * 64 + li] = c[0]; RED[wave * 64 + 16 + li] = c[1]; }
    }
    if (wave == 0) STAMP(18);
    WBAR();
    if (wave == 0) STAMP(19);
    if (tid < 16) {
      float g0 = 0.f, u0 = 0.f, g1 = 0.f, u1 = 0.f;
#pragma unroll
      for (int kq = 0; kq < 4; ++kq) {
        g0 += RED[(2 * kq) * 64 + tid]; u0 += RED[(2 * kq + 1) * 64 + tid];
        g1 += RED[(2 * kq) * 64 + 16 + tid]; u1 += RED[(2 * kq + 1) * 64 + 16 + tid];
      }
      const unsigned gp = pk2(g0, g1), up = pk2(u0, u1);
      const unsigned sp = pk2(1.0f / (1.0f + __expf(-lo(gp))), 1.0f / (1.0f + __expf(-hi(gp))));
      st_gran(GA + fb * 16 + tid, tag, pk2(lo(sp) * lo(up), hi(sp) * hi(up)));
    }
    if (wave == 0) STAMP(6);
    STAMP_FLUSH();
    return;
  }

  {
    asm volatile("s_waitcnt vmcnt(0)" ::: "memory");
    tr_frag8<1024, 128>(img_l + (wave * 224 + 8 * lg + (li >> 2)) * 32 + 8 * (li & 3), bf);
    const int cb = xc * 4 + (r - 22);
    float hres = 0.f;
    if (wave == 0) {
      unsigned v[8];
      const int q = (lane >> 4) & 1, c = lane & 15;
      sweep<8>(GH + cb * 16 + c, 512, tag, v, 8);
      float e[8];
#pragma unroll
      for (int k = 0; k < 8; ++k) e[k] = q ? hi(v[k]) : lo(v[k]);
      const float a0 = (e[0] + e[1]) + (e[2] + e[3]), a1 = (e[4] + e[5]) + (e[6] + e[7]);
      hres = rbf(XS[q * D + cb * 16 + c] + rbf(a0 + a1));
    }
    {
      unsigned v[4];
      sweep<4>(GA + wave * 224 + lane, 64, tag, v, lane < 32 ? 4 : 3);
      if (lane < 32) ((v4u*)(aw + 1024))[lane] = v4u{0u, 0u, 0u, 0u};
      u16* A0 = (u16*)aw; u16* A1 = (u16*)(aw + 512);
#pragma unroll
      for (int j = 0; j < 4; ++j) if (j < 3 || lane < 32) { A0[j * 64 + lane] = (u16)(v[j] & 0xffffu); A1[j * 64 + lane] = (u16)(v[j] >> 16); }
    }
    if (wave == 0) STAMP(5);
    LWAIT();
    {
      const unsigned char* ab = aw + (li < 2 ? li : 2) * 512 + lg * 16;
      f32x4 c = {0.f, 0.f, 0.f, 0.f};
#pragma unroll
      for (int ks = 0; ks < 7; ++ks) c = MFMA(*(const v4u*)(ab + ks * 64), bf[ks], c);
      if (lg == 0) { RED[wave * 64 + li] = c[0]; RED[wave * 64 + 16 + li] = c[1]; }
    }
    WBAR();
    if (tid < 32) {
      float a = 0.f;
#pragma unroll
      for (int w = 0; w < NW; ++w) a += RED[w * 64 + tid];
      const int q = tid >> 4, c = tid & 15;
      out[q * D + cb * 16 + c] = (u16)(pk2(hres + rbf(a), 0.f) & 0xffffu);
      if (cb == 0 && tid == 0) __hip_atomic_store((gu32*)(ws + WS_EPOCH), tag, RLX_AGENT);
    }
    if (wave == 0) STAMP(6);
    STAMP_FLUSH();
  }
}

extern "C" void kernel_launch(void* const* d_in, const int* in_sizes, int n_in, void* d_out, int out_size, void* d_ws, size_t ws_size,
                              hipStream_t stream) {
  (void)hipFuncSetAttribute((const void*)mega, hipFuncAttributeMaxDynamicSharedMemorySize, L_END);
  mega<<<256, NT, L_END, stream>>>((const u16*)d_in[0], (const u16*)d_in[1], (const u16*)d_in[2], (const u16*)d_in[3], (const u16*)d_in[4],
                                   (const u16*)d_in[6], (const u16*)d_in[7], (const u16*)d_in[8], (u16*)d_out, (unsigned char*)d_ws);
}
```
